```python
import torch

def _softmax(x, axis=-1):
    return torch.softmax(x, dim=axis)

def _gelu(x):
    return torch.nn.functional.gelu(x, approximate='tanh')

def _silu(x):
    return torch.nn.functional.silu(x)

def _sigmoid(x):
    return torch.sigmoid(x)

def _layernorm(x, axis=-1, eps=1e-05):
    m = x.mean(dim=axis, keepdim=True)
    v = x.var(dim=axis, keepdim=True, unbiased=False)
    return (x - m) / torch.sqrt(v + eps)

def _scatter_add(tgt, idx, src, axis=0):
    return tgt.clone().index_add_(axis, idx, src)

def _segment_sum(src, seg, n):
    out = torch.zeros((n,) + src.shape[1:], dtype=src.dtype)
    return out.index_add_(0, seg, src)

def _pad(x, pw):
    flat = tuple((v for pair in reversed(pw) for v in pair))
    return torch.nn.functional.pad(x, flat)
kernel_name = 'concat_heads__spectral_gfnet__memory_aug_recurrent_mem_tr__prompt'

def setup_inputs():

    _w = lambda a: (lambda t: t.to(torch.bfloat16) if t.is_floating_point() else t)(torch.as_tensor(a))

    def _A_setup():
        B, S, D = (1, 2048, 448)
        Df = D // 2 + 1
        return {'x': _w((lambda *s: torch.randn(*s, dtype=torch.bfloat16) * 0.05)(B, S, D)), 'w_r': _w((lambda *s: torch.randn(*s, dtype=torch.bfloat16) * 3.2)(S, Df) * 0.02), 'w_i': _w((lambda *s: torch.randn(*s, dtype=torch.bfloat16) * 3.2)(S, Df) * 0.02)}
    _A_in = _A_setup()

    def _B_setup():
        B, S, Dm = (1, 2048, 448)
        H, Dh = (16, 256)
        M, seg = (8, 32)
        seg = min(seg, S)
        S = S // seg * seg
        return {'x': _w((lambda *s: torch.randn(*s, dtype=torch.bfloat16) * 0.05)(B, S, Dm)), 'mem0': _w((lambda *s: torch.randn(*s, dtype=torch.bfloat16) * 0.4)(B, M, Dm)), 'Wq': _w((lambda *s: torch.randn(*s, dtype=torch.bfloat16) * 6.4)(Dm, H, Dh) * Dm ** (-0.5)), 'Wk': _w((lambda *s: torch.randn(*s, dtype=torch.bfloat16) * 6.4)(Dm, H, Dh) * Dm ** (-0.5)), 'Wv': _w((lambda *s: torch.randn(*s, dtype=torch.bfloat16) * 0.4)(Dm, H, Dh) * Dm ** (-0.5)), 'Wo': _w((lambda *s: torch.randn(*s, dtype=torch.bfloat16) * 0.4)(H, Dh, Dm) * (H * Dh) ** (-0.5)), 'seg_len': seg}
    _B_in = _B_setup()
    return {'A_x': _A_in['x'], 'A_w_r': _A_in['w_r'], 'A_w_i': _A_in['w_i'], 'B_x': _B_in['x'], 'B_mem0': _B_in['mem0'], 'B_Wq': _B_in['Wq'], 'B_Wk': _B_in['Wk'], 'B_Wv': _B_in['Wv'], 'B_Wo': _B_in['Wo'], 'B_seg_len': _B_in['seg_len']}

def reference(A_x, A_w_r, A_w_i, B_x, B_mem0, B_Wq, B_Wk, B_Wv, B_Wo, B_seg_len):

    def _A_ref(x, w_r, w_i):
        B, S, D = x.shape
        X = (lambda x, axes=(-2, -1): torch.fft.rfft2(x.float(), dim=axes))(x, axes=(-2, -1))
        W = w_r + 1j * w_i
        X = X * W
        y = (lambda x, s, axes=(-2, -1): torch.fft.irfft2(x, s=s, dim=axes).to(torch.bfloat16))(X, (S, D), axes=(-2, -1))
        return y

    def _B_ref(x, mem0, Wq, Wk, Wv, Wo, seg_len):
        B, S, Dm = x.shape
        H, Dh = (Wq.shape[1], Wq.shape[2])
        M = mem0.shape[1]
        scale = Dh ** (-0.5)
        n_seg = S // seg_len
        L = M + seg_len
        idx = torch.arange(L)
        mask = (idx[:, None] < M) | (idx[:, None] >= idx[None, :])
        mem = mem0
        outs = []
        for s in range(n_seg):
            xs = x[:, s * seg_len:(s + 1) * seg_len, :].clone()
            h = torch.concatenate([mem, xs], axis=1)
            q = torch.einsum('btd,dhe->bhte', h, Wq)
            k = torch.einsum('btd,dhe->bhte', h, Wk)
            v = torch.einsum('btd,dhe->bhte', h, Wv)
            scores = torch.einsum('bhqe,bhke->bhqk', q, k) * scale
            scores = torch.where(mask[None, None], scores, -1000000000.0)
            p = _softmax(scores, axis=-1)
            o = torch.einsum('bhqk,bhke->bhqe', p, v)
            o = torch.einsum('bhte,hed->btd', o, Wo)
            mem = o[:, :M, :].clone()
            outs.append(o[:, M:, :])
        return torch.concatenate(outs, axis=1)
    a = _A_ref(A_x, A_w_r, A_w_i)
    b = _B_ref(B_x, B_mem0, B_Wq, B_Wk, B_Wv, B_Wo, B_seg_len)
    return torch.concatenate([a, b], axis=-1)
```

```cpp
#pragma clang diagnostic ignored "-Wint-to-pointer-cast"
#include <hip/hip_runtime.h>
#include <hip/hip_bf16.h>
#include <stdint.h>

typedef unsigned v2u __attribute__((ext_vector_type(2)));
typedef unsigned v4u __attribute__((ext_vector_type(4)));
typedef float v4f __attribute__((ext_vector_type(4)));
typedef short v4s __attribute__((ext_vector_type(4)));
typedef __bf16 v8b __attribute__((ext_vector_type(8)));
typedef unsigned short u16;
typedef __amdgpu_buffer_rsrc_t rsrc_t;
#define LAS __attribute__((address_space(3)))
#define DI __device__ __forceinline__
#define RFL(x) __builtin_amdgcn_readfirstlane(x)

constexpr unsigned KB = 1024;
constexpr unsigned W_S = 4 * 32768, W_MX = 8 * 32768, W_PROBE = 12 * 32768, W_O = 512 * KB, W_MP = 1408 * KB;
constexpr unsigned F_GU = 1920 * KB, F_SV = 2048 * KB, F_MISC = 2176 * KB, F_MH = 2208 * KB, F_RDY = 2212 * KB, F_W = 2216 * KB;
constexpr unsigned F_Q = F_MISC, F_ABORT = F_MISC + 256, F_GEN = F_MISC + 384, F_X = F_MISC + 512, F_Q2 = F_MISC + 4096, F_T = F_MISC + 16384, F_F1 = F_T + 4096, F_F2 = F_T + 6144, F_STAT = F_MISC + 24576;
constexpr unsigned COMBO = 28 * 14 * 1024, SEGH = 28 * 1024;
constexpr unsigned O_WF = 2304 * KB, O_G = O_WF + 34 * COMBO, O_U = O_G + 64 * 16 * SEGH, O_P = O_U + 64 * 16 * SEGH + 4096,
                   O_UM = O_P + 64 * 40 * KB, O_Y1 = O_UM + 64 * 112 * KB, O_Y2 = O_Y1 + 448 * 4096, O_WT = O_Y2 + 448 * 4096, O_END = O_WT + 225 * 8192;
constexpr unsigned SENT = 0xFFFFFFFFu, OOB = 0x10000000u;
constexpr unsigned SPIN_MAX = 1u << 18;
#define STAG 0
#define FFTW 0
#define WWO 4
#define NPR 3
#define ROT 0
#define PRETOUCH 1
#define CODEPF 1
#define TLBPF 0
#define SLP 16
constexpr int LDS_BYTES = 160 * 1024;

DI int fresh_lane() { int l; asm volatile("v_mbcnt_lo_u32_b32 %0, -1, 0\n\tv_mbcnt_hi_u32_b32 %0, -1, %0" : "=v"(l)); return l & 63; }
typedef float v2f __attribute__((ext_vector_type(2)));
typedef __bf16 v2b __attribute__((ext_vector_type(2)));
DI unsigned cvtpk(float lo, float hi) { return __builtin_bit_cast(unsigned, __builtin_convertvector((v2f){lo, hi}, v2b)); }
DI v4f mfma(v4u a, v4u b, v4f c) { return __builtin_amdgcn_mfma_f32_16x16x32_bf16(__builtin_bit_cast(v8b, a), __builtin_bit_cast(v8b, b), c, 0, 0, 0); }
DI v4u pack8(v4f a, v4f b) { return (v4u){cvtpk(a.x, a.y), cvtpk(a.z, a.w), cvtpk(b.x, b.y), cvtpk(b.z, b.w)}; }
DI v4u ld16(rsrc_t r, unsigned v, unsigned s) { return __builtin_amdgcn_raw_buffer_load_b128(r, v, s, 16); }
DI void st16(rsrc_t r, v4u d, unsigned v, unsigned s) { __builtin_amdgcn_raw_buffer_store_b128(d, r, v, s, 16); }
DI void st8(rsrc_t r, v2u d, unsigned v, unsigned s) { __builtin_amdgcn_raw_buffer_store_b64(d, r, v, s, 16); }
DI void st4(rsrc_t r, unsigned d, unsigned v, unsigned s) { __builtin_amdgcn_raw_buffer_store_b32(d, r, v, s, 16); }
DI v2u trread(unsigned a) { return __builtin_bit_cast(v2u, __builtin_amdgcn_ds_read_tr16_b64_v4i16((LAS v4s*)a)); }
DI v4u lds16(unsigned a) { return *(LAS v4u*)a; }
#define WAIT_VM(n) asm volatile("s_waitcnt vmcnt(" #n ")" ::: "memory")
#define BAR() asm volatile("s_waitcnt lgkmcnt(0)\n\ts_barrier" ::: "memory")
DI void dma16(rsrc_t r, unsigned voff, unsigned soff, unsigned ldsdst) {
    asm volatile("s_mov_b32 m0, %0\n\ts_nop 3\n\tbuffer_load_dwordx4 %1, %2, %3 offen sc1 lds" :: "s"(ldsdst), "v"(voff), "s"(r), "s"(soff) : "memory");
}
struct Tag { unsigned lo, hi; };
DI unsigned wait_flags(rsrc_t rw, Tag t, unsigned base, unsigned stride, int n, int lane, bool lazy = false) {
    const unsigned off = base + (lane < n ? lane : 0) * stride;
    v2u f;
    for (unsigned sp = 0;; ++sp) {
        asm volatile("" ::: "memory");
        f = __builtin_amdgcn_raw_buffer_load_b64(rw, off, 0, 16);
        if (!__any(f.x != t.lo || (f.y ^ t.hi) > 4095u)) break;
        if (sp > SPIN_MAX) { st4(rw, 4u, F_ABORT, 0); break; }
        if (lazy) __builtin_amdgcn_s_sleep(SLP); else __builtin_amdgcn_s_sleep(3);
    }
    asm volatile("" ::: "memory");
    return f.y ^ t.hi;
}
DI void set_flag(rsrc_t rw, Tag t, unsigned flag, unsigned val) { st8(rw, (v2u){t.lo, t.hi ^ val}, flag, 0); }
DI void publish(rsrc_t rw, Tag t, unsigned flag) {
    WAIT_VM(0); BAR();
    if (RFL(threadIdx.x >> 6) == 0 && fresh_lane() == 0) set_flag(rw, t, flag, 0);
}
DI float xor16f(float x, bool add) { auto t = __builtin_amdgcn_permlane16_swap(__float_as_uint(x), __float_as_uint(x), false, false); const float a = __uint_as_float(t[0]), b = __uint_as_float(t[1]); return add ? a + b : fmaxf(a, b); }
DI float xor32f(float x, bool add) { auto t = __builtin_amdgcn_permlane32_swap(__float_as_uint(x), __float_as_uint(x), false, false); const float a = __uint_as_float(t[0]), b = __uint_as_float(t[1]); return add ? a + b : fmaxf(a, b); }

struct Args { const u16 *ax, *wr, *wi, *bx, *mem0, *wq, *wk, *wv, *wo; u16* out; char* ws; };
DI Args get_args() { const __attribute__((address_space(4))) Args* p = (const __attribute__((address_space(4))) Args*)__builtin_amdgcn_kernarg_segment_ptr(); asm volatile("" : "+s"(p));
    Args a; a.ax = p->ax; a.wr = p->wr; a.wi = p->wi; a.bx = p->bx; a.mem0 = p->mem0; a.wq = p->wq; a.wk = p->wk; a.wv = p->wv; a.wo = p->wo; a.out = p->out; a.ws = p->ws; return a; }

DI int pair_row(int tile, int p) { return 32 * (tile >> 1) + 8 * (p >> 2) + (p & 3) + 4 * (tile & 1); }
struct FoldSrc { rsrc_t ra, rb; };
template <bool VO> DI FoldSrc fold_src() { const Args a = get_args();
    return FoldSrc{__builtin_amdgcn_make_buffer_rsrc((void*)(VO ? a.wv : a.wk), 0, 448 * 8192, 0x00020000), __builtin_amdgcn_make_buffer_rsrc((void*)(VO ? a.wo : a.wq), 0, 448 * 8192, 0x00020000)}; }
template <bool VO> DI void fold_dma(const FoldSrc& f, int n, int h, int kb0, int nt0, unsigned ldsb, int w, int lane) {
    if (n < 8) { const int id = w * 8 + n, e = id * 8 + (lane >> 3), row = 2 * n + (lane >> 5);
        if (VO) dma16(f.rb, (lane >> 3) * 896 + ((lane & 7) ^ (2 * (e & 3))) * 16, (h * 256 + id * 8) * 896 + nt0 * 32, ldsb + id * 1024);
        else dma16(f.rb, pair_row(0, row) * 8192 + ((lane & 31) ^ row) * 16, pair_row(nt0 + w, 0) * 8192 + h * 512, ldsb + id * 1024);
    } else { const int k = n - 8, r = 2 * (k & 15) + (lane >> 5), key = ((r >> 3) << 2) | (r & 3);
        if (k < 16 || w < 3) dma16(f.ra, (lane >> 5) * 8192 + ((lane & 31) ^ key) * 16, (kb0 + w + 4 * (k >> 4)) * (32 * 8192) + (k & 15) * 16384 + h * 512, ldsb + 32768 + (w + 4 * (k >> 4)) * 16384 + (k & 15) * 1024); }
}
DI void fold_start(int h, int kb0, int nt0, unsigned ldsb) {
    const int lane = fresh_lane(), w = RFL(threadIdx.x >> 6);
    const FoldSrc f = fold_src<false>();
#pragma unroll
    for (int n = 0; n < 40; ++n) fold_dma<false>(f, n, h, kb0, nt0, ldsb, w, lane);
}
template <bool VO> DI void fold_pass(rsrc_t rw, int h, int kb0, int nt0, unsigned ldsb) {
    const int lane = fresh_lane(), w = RFL(threadIdx.x >> 6), l15 = lane & 15, g = lane >> 4;
    WAIT_VM(0); BAR();
    v4u X[2][8], Y[2][8], B[4][8];
#pragma unroll
    for (int es = 0; es < 8; ++es) {
#pragma unroll
        for (int j = 0; j < 2; ++j) { const unsigned ad = ldsb + 32768 + (w < 3 ? w + 4 * j : w) * 16384 + (8 * (l15 >> 2) + (l15 & 3)) * 512 + ((4 * es + g) ^ l15) * 16; X[j][es] = lds16(ad); Y[j][es] = lds16(ad + 2048); }
#pragma unroll
        for (int t = 0; t < 4; ++t) {
            if (VO) { const unsigned ad = ldsb + (32 * es + 8 * g + (l15 >> 2)) * 128 + (lane & 3) * 8 + (t ^ (l15 >> 2)) * 32; const v2u lo = trread(ad), hi = trread(ad + 512); B[t][es] = (v4u){lo.x, lo.y, hi.x, hi.y}; }
            else B[t][es] = lds16(ldsb + l15 * 512 + t * 8192 + ((4 * es + g) ^ l15) * 16);
        }
    }
    FoldSrc f2; if (!VO) { f2 = fold_src<true>(); BAR(); }
    const float sc = VO ? 1.0f : 1.4426950408889634f / 16.0f;
#pragma unroll
    for (int t = 0; t < 4; ++t) {
        v4f c0 = {0, 0, 0, 0}, c1 = c0, c2 = c0, c3 = c0;
#pragma unroll
        for (int es = 0; es < 8; ++es) {
            c0 = mfma(X[0][es], B[t][es], c0); c1 = mfma(Y[0][es], B[t][es], c1); c2 = mfma(X[1][es], B[t][es], c2); c3 = mfma(Y[1][es], B[t][es], c3);
            if (!VO) { fold_dma<true>(f2, t * 10 + es, h, kb0, nt0, ldsb, w, lane); if (es >= 6) fold_dma<true>(f2, t * 10 + es + 2, h, kb0, nt0, ldsb, w, lane); __builtin_amdgcn_sched_barrier(0); }
        }
        const unsigned so = O_WF + (2 * h + VO) * COMBO + ((nt0 + t) * 14 + kb0 + w) * 1024;
        st16(rw, VO ? pack8(c0, c1) : pack8(c0 * sc, c1 * sc), lane * 16, so);
        if (w < 3) st16(rw, VO ? pack8(c2, c3) : pack8(c2 * sc, c3 * sc), lane * 16, so + 4096);
    }
}

constexpr int NA = 60;
struct Wreg { v4u a[NA]; v4u v[98 - NA]; };
template <bool PAIR> DI void load_w(Wreg& W, rsrc_t rw, int combo, int w, int lane) {
#pragma unroll
    for (int ks = 0; ks < 14; ++ks)
#pragma unroll
        for (int t = 0; t < 7; ++t) {
            const v4u x = ld16(rw, lane * 16, O_WF + combo * COMBO + (PAIR ? (t < 6 ? 6 * w + t : 24 + w) : 7 * w + t) * 14336 + ks * 1024);
            const int f = t * 14 + ks; if (f < NA) W.a[f] = x; else W.v[f - NA] = x; }
}
template <bool WA, bool FIRST, int F> DI void wmfma(v4f& c, const v4u& x, const Wreg& W) {
    if constexpr (F < NA) {
        if constexpr (WA) { if constexpr (FIRST) asm volatile("v_mfma_f32_16x16x32_bf16 %0, %1, %2, 0" : "=&v"(c) : "a"(W.a[F]), "v"(x)); else asm volatile("v_mfma_f32_16x16x32_bf16 %0, %1, %2, %0" : "+v"(c) : "a"(W.a[F]), "v"(x)); }
        else { if constexpr (FIRST) asm volatile("v_mfma_f32_16x16x32_bf16 %0, %1, %2, 0" : "=&v"(c) : "v"(x), "a"(W.a[F])); else asm volatile("v_mfma_f32_16x16x32_bf16 %0, %1, %2, %0" : "+v"(c) : "v"(x), "a"(W.a[F])); }
    } else {
        if constexpr (WA) { if constexpr (FIRST) asm volatile("v_mfma_f32_16x16x32_bf16 %0, %1, %2, 0" : "=&v"(c) : "v"(W.v[F - NA]), "v"(x)); else asm volatile("v_mfma_f32_16x16x32_bf16 %0, %1, %2, %0" : "+v"(c) : "v"(W.v[F - NA]), "v"(x)); }
        else { if constexpr (FIRST) asm volatile("v_mfma_f32_16x16x32_bf16 %0, %1, %2, 0" : "=&v"(c) : "v"(x), "v"(W.v[F - NA])); else asm volatile("v_mfma_f32_16x16x32_bf16 %0, %1, %2, %0" : "+v"(c) : "v"(x), "v"(W.v[F - NA])); }
    }
}
template <bool WA, int KS> DI void kstep2(v4f (&c)[2][7], const v4u& x0, const v4u& x1, const Wreg& W) {
    wmfma<WA, KS == 0, 0 * 14 + KS>(c[0][0], x0, W); wmfma<WA, KS == 0, 0 * 14 + KS>(c[1][0], x1, W);
    wmfma<WA, KS == 0, 1 * 14 + KS>(c[0][1], x0, W); wmfma<WA, KS == 0, 1 * 14 + KS>(c[1][1], x1, W);
    wmfma<WA, KS == 0, 2 * 14 + KS>(c[0][2], x0, W); wmfma<WA, KS == 0, 2 * 14 + KS>(c[1][2], x1, W);
    wmfma<WA, KS == 0, 3 * 14 + KS>(c[0][3], x0, W); wmfma<WA, KS == 0, 3 * 14 + KS>(c[1][3], x1, W);
    wmfma<WA, KS == 0, 4 * 14 + KS>(c[0][4], x0, W); wmfma<WA, KS == 0, 4 * 14 + KS>(c[1][4], x1, W);
    wmfma<WA, KS == 0, 5 * 14 + KS>(c[0][5], x0, W); wmfma<WA, KS == 0, 5 * 14 + KS>(c[1][5], x1, W);
    wmfma<WA, KS == 0, 6 * 14 + KS>(c[0][6], x0, W); wmfma<WA, KS == 0, 6 * 14 + KS>(c[1][6], x1, W);
}
template <bool WA, int KS> DI void kstep1(v4f (&c)[7], const v4u& x0, const Wreg& W) {
    wmfma<WA, KS == 0, 0 * 14 + KS>(c[0], x0, W); wmfma<WA, KS == 0, 1 * 14 + KS>(c[1], x0, W); wmfma<WA, KS == 0, 2 * 14 + KS>(c[2], x0, W); wmfma<WA, KS == 0, 3 * 14 + KS>(c[3], x0, W);
    wmfma<WA, KS == 0, 4 * 14 + KS>(c[4], x0, W); wmfma<WA, KS == 0, 5 * 14 + KS>(c[5], x0, W); wmfma<WA, KS == 0, 6 * 14 + KS>(c[6], x0, W);
}
template <int MODE> struct XF { v4u x0, x1; v2u p, q, r, s; };
template <int MODE, int KS> DI void xfetch(XF<MODE>& f, unsigned ad) {
    if constexpr (MODE == 0 || MODE == 3) asm volatile("ds_read_b128 %0, %2 offset:%3\n\tds_read_b128 %1, %2 offset:%4" : "=&v"(f.x0), "=&v"(f.x1) : "v"(ad ^ ((KS & 1) * 64)), "i"((KS >> 1) * 128), "i"((MODE == 0 ? 14336 : 3584) + (KS >> 1) * 128) : "memory");
    else if constexpr (MODE == 1)
        asm volatile("ds_read_b64_tr_b16 %0, %4 offset:%5\n\tds_read_b64_tr_b16 %1, %4 offset:%6\n\tds_read_b64_tr_b16 %2, %4 offset:%7\n\tds_read_b64_tr_b16 %3, %4 offset:%8"
                     : "=&v"(f.p), "=&v"(f.q), "=&v"(f.r), "=&v"(f.s) : "v"(ad), "i"(KS * 2048), "i"(KS * 2048 + 256), "i"(KS * 2048 + 32), "i"(KS * 2048 + 288) : "memory");
    else asm volatile("ds_read2_b32 %0, %2 offset0:0 offset1:32\n\tds_read2_b32 %1, %2 offset0:1 offset1:33" : "=&v"(f.p), "=&v"(f.q) : "v"(ad + KS * 512) : "memory");
}
template <int MODE, bool LAST> DI void xwait(XF<MODE>& f) {
    if constexpr (MODE == 0 || MODE == 3) asm volatile("s_waitcnt lgkmcnt(%2)" : "+v"(f.x0), "+v"(f.x1) : "i"(LAST ? 0 : 2) : "memory");
    else if constexpr (MODE == 1) { asm volatile("s_waitcnt lgkmcnt(%4)" : "+v"(f.p), "+v"(f.q), "+v"(f.r), "+v"(f.s) : "i"(LAST ? 0 : 4) : "memory");
        f.x0 = (v4u){f.p.x, f.p.y, f.q.x, f.q.y}; f.x1 = (v4u){f.r.x, f.r.y, f.s.x, f.s.y}; asm volatile("s_nop 1" : "+v"(f.x0), "+v"(f.x1)); }
    else { asm volatile("s_waitcnt lgkmcnt(%2)" : "+v"(f.p), "+v"(f.q) : "i"(LAST ? 0 : 2) : "memory");
        f.x0 = (v4u){f.p.x, f.p.y, f.q.x, f.q.y}; asm volatile("s_nop 1" : "+v"(f.x0)); }
}
template <bool WA, int MODE, int KS> DI void gemm2_from(v4f (&c)[2][7], XF<MODE>& f, unsigned ad, const Wreg& W) {
    XF<MODE> n;
    if constexpr (KS < 13) xfetch<MODE, KS + 1>(n, ad);
    xwait<MODE, KS == 13>(f);
    kstep2<WA, KS>(c, f.x0, f.x1, W);
    if constexpr (KS < 13) gemm2_from<WA, MODE, KS + 1>(c, n, ad, W);
}
template <bool WA, int MODE> DI void gemm2(v4f (&c)[2][7], unsigned ad, const Wreg& W) {
    XF<MODE> f; xfetch<MODE, 0>(f, ad); gemm2_from<WA, MODE, 0>(c, f, ad, W);
    asm volatile("s_nop 7" : "+v"(c[0][0]), "+v"(c[0][1]), "+v"(c[0][2]), "+v"(c[0][3]), "+v"(c[0][4]), "+v"(c[0][5]), "+v"(c[0][6]),
                 "+v"(c[1][0]), "+v"(c[1][1]), "+v"(c[1][2]), "+v"(c[1][3]), "+v"(c[1][4]), "+v"(c[1][5]), "+v"(c[1][6]));
}
template <bool WA, int KS> DI void gemm1_from(v4f (&c)[7], XF<2>& f, unsigned ad, const Wreg& W) {
    XF<2> n;
    if constexpr (KS < 13) xfetch<2, KS + 1>(n, ad);
    xwait<2, KS == 13>(f);
    kstep1<WA, KS>(c, f.x0, W);
    if constexpr (KS < 13) gemm1_from<WA, KS + 1>(c, n, ad, W);
}
template <bool WA> DI void gemm1(v4f (&c)[7], unsigned ad, const Wreg& W) {
    XF<2> f; xfetch<2, 0>(f, ad); gemm1_from<WA, 0>(c, f, ad, W);
    asm volatile("s_nop 7" : "+v"(c[0]), "+v"(c[1]), "+v"(c[2]), "+v"(c[3]), "+v"(c[4]), "+v"(c[5]), "+v"(c[6]));
}
template <bool PERM> DI void dma_rows(rsrc_t rx, int seg, unsigned dst, int w, int lane) {
#pragma unroll
    for (int k = 0; k < 7; ++k) { const int i = (w * 7 + k) * 64 + lane, r = i / 56, c = i - r * 56, key = PERM ? ((r >> 3) << 1) | ((r >> 1) & 1) : (r >> 1) & 7;
        dma16(rx, r * 896 + (c ^ key) * 16, seg * SEGH, dst + (w * 7 + k) * 1024); }
}
template <bool PERM> DI unsigned xaddr(unsigned base, int lane) { const int p = lane & 15; return base + (PERM ? 8 * (p >> 2) + (p & 3) : p) * 896 + (((lane >> 4) ^ (p >> 1)) * 16); }
DI v4u xfrag(unsigned xa, int tile, int ks) { return lds16((xa ^ ((ks & 1) * 64)) + tile * 14336 + (ks >> 1) * 128); }
DI void fetch_mem(rsrc_t rw, Tag tag, int s, unsigned dst) {
    const int tid = RFL(threadIdx.x >> 6) * 64 + fresh_lane(); const unsigned p1 = 256 + (tid < 192 ? tid : tid - 64);
    if (tid < 64) wait_flags(rw, tag, F_MH + min(s, 62) * 64, 0, 1, tid, true);
    asm volatile("s_barrier" ::: "memory");
    v4u m0, m1;
    for (unsigned sp = 0;; ++sp) {
        asm volatile("" ::: "memory");
        m0 = ld16(rw, tid * 16, W_MP + s * 8192); m1 = ld16(rw, p1 * 16, W_MP + s * 8192);
        const unsigned mx = max(max(max(m0.x, m0.y), max(m0.z, m0.w)), max(max(m1.x, m1.y), max(m1.z, m1.w)));
        if (!__any(mx == SENT)) break;
        if (sp > SPIN_MAX) { st4(rw, 5u, F_ABORT, 0); break; }
        __builtin_amdgcn_s_sleep(1);
    }
    *(LAS v4u*)(dst + tid * 16) = m0; *(LAS v4u*)(dst + p1 * 16) = m1;
}

constexpr unsigned L_X = 0, L_RED = 3 * SEGH, L_M = L_RED + 12288, L_SX = L_M + 7168;
template <bool QK> DI void worker_body(rsrc_t rw, Tag tag, int combo, int rr, unsigned ldsb) {
    const Args a = get_args();
    const int lane = fresh_lane(), w = RFL(threadIdx.x >> 6), l15 = lane & 15, g = lane >> 4, h = combo >> 1;
    const rsrc_t rx = __builtin_amdgcn_make_buffer_rsrc((void*)a.bx, 0, 2048 * 896, 0x00020000);
    BAR();
    dma_rows<false>(rx, rr, ldsb + L_X, w, lane);
    wait_flags(rw, tag, F_W + ((combo & 1) * 224 + h * 14) * 64, 64, 14, lane);
    WAIT_VM(0); BAR();
    for (int i = 0; i < rr * STAG; ++i) __builtin_amdgcn_s_sleep(8);
    Wreg W;
    load_w<QK>(W, rw, combo, w, lane);
    const unsigned ma = ldsb + L_M + (g >> 1) * 256 + (l15 & 7) * 16 + (g & 1) * 8;
    LAS v4f* red = (LAS v4f*)(ldsb + L_RED);
    int buf = 0;
    for (int k = 0; k <= 16; ++k) {
        const int nb = buf == 2 ? 0 : buf + 1, pb = buf == 0 ? 2 : buf - 1;
        if (k < 16) {
            const int s = rr + 4 * k;
            if (k < 15) dma_rows<false>(rx, s + 4, ldsb + L_X + nb * SEGH, w, lane);
            const unsigned xa = xaddr<false>(ldsb + L_X + buf * SEGH, lane);
            v4f c[2][7];
            gemm2<QK, 0>(c, xa, W);
            if (k == 0) {
            }
            if constexpr (!QK) {
#pragma unroll
                for (int t = 0; t < 7; ++t) st16(rw, pack8(c[0][t], c[1][t]), lane * 16, O_U + (s * 16 + h) * SEGH + (7 * w + t) * 1024);
            } else {
                v4f s00 = {0, 0, 0, 0}, s01 = s00, s11 = s00;
#pragma unroll
                for (int b = 0; b < 4; ++b) {
                    const int be = b < 3 ? 3 * w + b : 12 + (w >> 1);
                    v4u g0, g1;
                    const unsigned so = O_G + (s * 16 + h) * SEGH + be * 1024;
                    if (b < 3) { g0 = pack8(c[0][2 * b], c[0][2 * b + 1]); g1 = pack8(c[1][2 * b], c[1][2 * b + 1]); st16(rw, g0, lane * 16, so); st16(rw, g1, lane * 16, so + 14336); }
                    else { const v2u p0 = {cvtpk(c[0][6].x, c[0][6].y), cvtpk(c[0][6].z, c[0][6].w)}, p1 = {cvtpk(c[1][6].x, c[1][6].y), cvtpk(c[1][6].z, c[1][6].w)};
                        st8(rw, p0, lane * 16 + (w & 1) * 8, so); st8(rw, p1, lane * 16 + (w & 1) * 8, so + 14336);
                        g0 = (w & 1) ? (v4u){0, 0, p0.x, p0.y} : (v4u){p0.x, p0.y, 0, 0}; g1 = (w & 1) ? (v4u){0, 0, p1.x, p1.y} : (v4u){p1.x, p1.y, 0, 0}; }
                    const v4u x0 = xfrag(xa, 0, be), x1 = xfrag(xa, 1, be);
                    s00 = mfma(g0, x0, s00); s01 = mfma(g0, x1, s01); s11 = mfma(g1, x1, s11);
                }
                red[(w * 3 + 0) * 64 + lane] = s00; red[(w * 3 + 1) * 64 + lane] = s01; red[(w * 3 + 2) * 64 + lane] = s11;
                BAR();
                if (w < 3) *(LAS v4f*)(ldsb + L_SX + ((k & 1) * 3 + w) * 1024 + lane * 16) = (red[w * 64 + lane] + red[(3 + w) * 64 + lane]) + (red[(6 + w) * 64 + lane] + red[(9 + w) * 64 + lane]);
            }
            publish(rw, tag, F_GU + (s * 32 + combo) * 64);
            if (k == 0) {
            }
        }
        if (k > 0) {
            const int s = rr + 4 * (k - 1);
            fetch_mem(rw, tag, s, ldsb + L_M);
            BAR();
            v4f c[7];
            gemm1<QK>(c, ma, W);
            if constexpr (!QK) {
                if (lane < 32)
#pragma unroll
                    for (int t = 0; t < 7; ++t) st8(rw, (v2u){cvtpk(c[t].x, c[t].y), cvtpk(c[t].z, c[t].w)}, (h & 3) * 256 + l15 * 16 + g * 8, O_UM + ((s * 4 + (h >> 2)) * 28 + 7 * w + t) * 1024);
            } else {
                v4f m0 = {0, 0, 0, 0}, m1 = m0;
                const unsigned xa = xaddr<false>(ldsb + L_X + pb * SEGH, lane);
#pragma unroll
                for (int b = 0; b < 4; ++b) {
                    const int be = b < 3 ? 3 * w + b : 12 + (w >> 1);
                    v4u t0;
                    if (b < 3) t0 = pack8(c[2 * b], c[2 * b + 1]);
                    else { const unsigned p = cvtpk(c[6].x, c[6].y), q = cvtpk(c[6].z, c[6].w); t0 = (w & 1) ? (v4u){0, 0, p, q} : (v4u){p, q, 0, 0}; }
                    const v4u x0 = xfrag(xa, 0, be), x1 = xfrag(xa, 1, be);
                    m0 = mfma(t0, x0, m0); m1 = mfma(t0, x1, m1);
                }
                red[(w * 2 + 0) * 64 + lane] = m0; red[(w * 2 + 1) * 64 + lane] = m1;
                BAR();
                if (w < 2) {
                    const v4f sm = (red[w * 64 + lane] + red[(2 + w) * 64 + lane]) + (red[(4 + w) * 64 + lane] + red[(6 + w) * 64 + lane]);
                    const unsigned sxa = ldsb + L_SX + ((k - 1) & 1) * 3072 + lane * 16;
                    const v4f sx0 = *(LAS v4f*)(sxa + w * 1024), sx1 = *(LAS v4f*)(sxa + 2048);
                    const float NEG = -__builtin_inff();
                    float e[12];
#pragma unroll
                    for (int r = 0; r < 4; ++r) { const bool diag = 4 * g + r <= l15;
                        e[r] = (w == 1 || diag) ? sx0[r] : NEG; e[4 + r] = (w == 1 && diag) ? sx1[r] : NEG; e[8 + r] = g < 2 ? sm[r] : NEG; }
                    float mx = e[0];
#pragma unroll
                    for (int r = 1; r < 12; ++r) mx = fmaxf(mx, e[r]);
                    mx = xor32f(xor16f(mx, false), false);
                    float su = 0;
#pragma unroll
                    for (int r = 0; r < 12; ++r) { e[r] = __builtin_amdgcn_exp2f(e[r] - mx); su += e[r]; }
                    su = xor32f(xor16f(su, true), true);
                    const float rl = 1.0f / su;
                    st16(rw, (v4u){cvtpk(e[0] * rl, e[1] * rl), cvtpk(e[2] * rl, e[3] * rl), cvtpk(e[4] * rl, e[5] * rl), cvtpk(e[6] * rl, e[7] * rl)}, lane * 16, O_P + s * 40960 + (h * 2 + w) * 1024);
                    if (lane < 32) st8(rw, (v2u){cvtpk(e[8] * rl, e[9] * rl), cvtpk(e[10] * rl, e[11] * rl)}, (h & 3) * 256 + l15 * 16 + g * 8, O_P + s * 40960 + 32768 + ((h >> 2) * 2 + w) * 1024);
                }
            }
            publish(rw, tag, F_SV + (s * 32 + combo) * 64);
        }
        buf = nb;
    }
}

DI void pv_role(rsrc_t rw, Tag tag, int pi, unsigned ldsb) {
    const Args a = get_args();
    const int lane = fresh_lane(), w = RFL(threadIdx.x >> 6), tid = w * 64 + lane, l15 = lane & 15, g = lane >> 4;
    const int grp = pi / 7, ct = (pi % 7) * 4 + w;
    if (grp >= 4) return;
    const rsrc_t ro = __builtin_amdgcn_make_buffer_rsrc(a.out, 0, 2048 * 1792, 0x00020000);
    for (int s = grp; s < 64; s += 4) {
        wait_flags(rw, tag, F_GU + (s * 32 + 1) * 64, 128, 16, lane, true);
        v4u U[20];
#pragma unroll
        for (int h = 0; h < 16; ++h) U[h] = ld16(rw, lane * 16, O_U + (s * 16 + h) * SEGH + ct * 1024);
        wait_flags(rw, tag, F_SV + s * 32 * 64, 64, 32, lane, s < 62);
#pragma unroll
        for (int k = 0; k < 10; ++k) dma16(rw, lane * 16, O_P + s * 40960 + (w * 10 + k) * 1024, ldsb + (w * 10 + k) * 1024);
#pragma unroll
        for (int q = 0; q < 4; ++q) U[16 + q] = ld16(rw, lane * 16, O_UM + ((s * 4 + q) * 28 + ct) * 1024);
        WAIT_VM(0); BAR();
        v4f c0 = {0, 0, 0, 0}, c1 = c0;
#pragma unroll
        for (int k = 0; k < 20; ++k) { c0 = mfma(U[k], lds16(ldsb + (2 * k) * 1024 + lane * 16), c0); c1 = mfma(U[k], lds16(ldsb + (2 * k + 1) * 1024 + lane * 16), c1); }
        const unsigned o = (l15 * 896 + 448 + 16 * ct + 4 * g) * 2;
        st8(ro, (v2u){cvtpk(c0.x, c0.y), cvtpk(c0.z, c0.w)}, o, s * (32 * 1792));
        st8(ro, (v2u){cvtpk(c1.x, c1.y), cvtpk(c1.z, c1.w)}, o + 16 * 1792, s * (32 * 1792));
        BAR();
    }
}

DI float cosr(int m, int n) { return __builtin_amdgcn_cosf((float)m * (1.0f / (float)n)); }
DI float sinr(int m, int n) { return __builtin_amdgcn_sinf((float)m * (1.0f / (float)n)); }
DI void fft_tables(rsrc_t rw, Tag tag, int fi, unsigned ldsb) {
    const Args a = get_args();
    const int tid = RFL(threadIdx.x >> 6) * 64 + fresh_lane();
    for (int i = fi * 256 + tid; i < 2 * 392 * 64; i += 64 * 256) {
        const int inv = i / (392 * 64), r = i % (392 * 64), ln = r & 63, ks = (r >> 6) % 14, nt = (r >> 6) / 14;
        const int n = inv ? pair_row(nt, ln & 15) : nt * 16 + (ln & 15), k0 = ks * 32 + (ln >> 4) * 8;
        float v[8];
#pragma unroll
        for (int e = 0; e < 8; ++e) { const int col = inv ? k0 + e : n, d = inv ? n : k0 + e, f = col == 1 ? 224 : col >> 1, m = (f * d) % 448;
            const float t = (col > 1 && (col & 1)) ? -sinr(m, 448) : cosr(m, 448); v[e] = (inv && col > 1) ? 2.0f * t : t; }
        st16(rw, (v4u){cvtpk(v[0], v[1]), cvtpk(v[2], v[3]), cvtpk(v[4], v[5]), cvtpk(v[6], v[7])}, r * 16, O_WF + (32 + inv) * COMBO);
    }
    for (int i = tid; i < 900; i += 256) { *(LAS v4u*)(ldsb + i * 16) = ((const v4u*)(a.wr + fi * 32 * 225))[i]; *(LAS v4u*)(ldsb + 14400 + i * 16) = ((const v4u*)(a.wi + fi * 32 * 225))[i]; }
    __syncthreads();
    for (int i = tid; i < 225 * 8; i += 256) { const int c = i >> 3, r4 = (i & 7) * 4; unsigned o[4];
#pragma unroll
        for (int k = 0; k < 4; ++k) o[k] = (unsigned)*(LAS u16*)(ldsb + ((r4 + k) * 225 + c) * 2) | ((unsigned)*(LAS u16*)(ldsb + 14400 + ((r4 + k) * 225 + c) * 2) << 16);
        st16(rw, (v4u){o[0], o[1], o[2], o[3]}, c * 8192 + (fi * 32 + r4) * 4, O_WT); }
    publish(rw, tag, F_T + fi * 64);
}
DI void fft_columns(rsrc_t rw, int fi, unsigned ldsb) {
    const int lane = fresh_lane(), w = RFL(threadIdx.x >> 6), tid = w * 64 + lane, l15 = lane & 15, g = lane >> 4;
    const unsigned lin = ldsb + w * 16896, ltr = lin + 8192;
    v4u C32[2], S32[2], N32[2], C64[4][2], S64[4][2], N64[4][2];
    float twc[4][2][4], tws[4][2][4];
#pragma unroll
    for (int kt = 0; kt < 2; ++kt) { float c[8], s[8];
#pragma unroll
        for (int e = 0; e < 8; ++e) { const int m = ((16 * kt + l15) * (8 * g + e)) & 31; c[e] = cosr(m, 32); s[e] = sinr(m, 32); }
        C32[kt] = (v4u){cvtpk(c[0], c[1]), cvtpk(c[2], c[3]), cvtpk(c[4], c[5]), cvtpk(c[6], c[7])}; S32[kt] = (v4u){cvtpk(s[0], s[1]), cvtpk(s[2], s[3]), cvtpk(s[4], s[5]), cvtpk(s[6], s[7])};
        N32[kt] = S32[kt] ^ 0x80008000u; }
#pragma unroll
    for (int ut = 0; ut < 4; ++ut)
#pragma unroll
        for (int m2 = 0; m2 < 2; ++m2) { float c[8], s[8];
#pragma unroll
            for (int e = 0; e < 8; ++e) { const int m = ((16 * ut + l15) * (32 * m2 + 16 * (e >> 2) + 4 * g + (e & 3))) & 63; c[e] = cosr(m, 64); s[e] = sinr(m, 64); }
            C64[ut][m2] = (v4u){cvtpk(c[0], c[1]), cvtpk(c[2], c[3]), cvtpk(c[4], c[5]), cvtpk(c[6], c[7])}; S64[ut][m2] = (v4u){cvtpk(s[0], s[1]), cvtpk(s[2], s[3]), cvtpk(s[4], s[5]), cvtpk(s[6], s[7])};
            N64[ut][m2] = S64[ut][m2] ^ 0x80008000u;
#pragma unroll
            for (int r = 0; r < 4; ++r) { const int m = (16 * ut + 4 * g + r) * (16 * m2 + l15); twc[ut][m2][r] = cosr(m, 2048); tws[ut][m2][r] = sinr(m, 2048); } }
    const float scale = 1.0f / (2048.0f * 448.0f);
    const v4f Z4 = {0, 0, 0, 0};
    for (int kc = fi * 4 + w; kc < 225; kc += 128) {
        const bool cplx = kc != 0 && kc != 224;
        const int rre = cplx ? 2 * kc : (kc ? 1 : 0);
#pragma unroll
        for (int k = 0; k < 4; ++k) dma16(rw, lane * 16, O_Y1 + rre * 4096 + k * 1024, lin + k * 1024);
        if (cplx) {
#pragma unroll
            for (int k = 0; k < 4; ++k) dma16(rw, lane * 16, O_Y1 + (rre + 1) * 4096 + k * 1024, lin + 4096 + k * 1024);
        } else {
#pragma unroll
            for (int k = 0; k < 4; ++k) *(LAS v4u*)(lin + 4096 + k * 1024 + lane * 16) = (v4u){0, 0, 0, 0};
        }
        unsigned wf[4][2][4];
#pragma unroll
        for (int ut = 0; ut < 4; ++ut)
#pragma unroll
            for (int kt = 0; kt < 2; ++kt)
#pragma unroll
                for (int r = 0; r < 4; ++r) wf[ut][kt][r] = __builtin_amdgcn_raw_buffer_load_b32(rw, (l15 + 128 * g) * 4, O_WT + kc * 8192 + (16 * kt + 32 * (16 * ut + r)) * 4, 16);
        asm volatile("s_waitcnt vmcnt(0) lgkmcnt(0)" ::: "memory");
        v4u Br[2][2], Bi[2][2];
#pragma unroll
        for (int m2 = 0; m2 < 2; ++m2) { v4f zr[2][2], zi[2][2];
#pragma unroll
            for (int y = 0; y < 2; ++y) { const int st = 2 * m2 + y; const unsigned ad = lin + (8 * g + (l15 >> 2)) * 128 + st * 32 + (lane & 3) * 8;
                const v2u p = trread(ad), q = trread(ad + 512), r = trread(ad + 4096), s = trread(ad + 4608);
                const v4u xr = {p.x, p.y, q.x, q.y}, xi = {r.x, r.y, s.x, s.y};
#pragma unroll
                for (int kt = 0; kt < 2; ++kt) { const v4f ar = mfma(xi, S32[kt], mfma(xr, C32[kt], Z4)), ai = mfma(xi, C32[kt], mfma(xr, N32[kt], Z4));
#pragma unroll
                    for (int r2 = 0; r2 < 4; ++r2) { zr[y][kt][r2] = ar[r2] * twc[st][kt][r2] + ai[r2] * tws[st][kt][r2]; zi[y][kt][r2] = ai[r2] * twc[st][kt][r2] - ar[r2] * tws[st][kt][r2]; } } }
#pragma unroll
            for (int kt = 0; kt < 2; ++kt) { Br[m2][kt] = pack8(zr[0][kt], zr[1][kt]); Bi[m2][kt] = pack8(zi[0][kt], zi[1][kt]); } }
        v4u Vr[2][2], Vi[2][2];
#pragma unroll
        for (int m2 = 0; m2 < 2; ++m2)
#pragma unroll
            for (int kt = 0; kt < 2; ++kt) { v4f vr[2], vi[2];
#pragma unroll
                for (int y = 0; y < 2; ++y) { const int ut = 2 * m2 + y;
                    const v4f yr = mfma(S64[ut][1], Bi[1][kt], mfma(S64[ut][0], Bi[0][kt], mfma(C64[ut][1], Br[1][kt], mfma(C64[ut][0], Br[0][kt], Z4))));
                    const v4f yi = mfma(C64[ut][1], Bi[1][kt], mfma(C64[ut][0], Bi[0][kt], mfma(N64[ut][1], Br[1][kt], mfma(N64[ut][0], Br[0][kt], Z4))));
#pragma unroll
                    for (int r = 0; r < 4; ++r) { const float fr = __uint_as_float(wf[ut][kt][r] << 16) * scale, fm = __uint_as_float(wf[ut][kt][r] & 0xffff0000u) * scale;
                        vr[y][r] = yr[r] * fr - yi[r] * fm; vi[y][r] = yr[r] * fm + yi[r] * fr; } }
                Vr[m2][kt] = pack8(vr[0], vr[1]); Vi[m2][kt] = pack8(vi[0], vi[1]); }
#pragma unroll
        for (int st = 0; st < 4; ++st)
#pragma unroll
            for (int kt = 0; kt < 2; ++kt) {
                const v4f rr = mfma(N64[st][1], Vi[1][kt], mfma(N64[st][0], Vi[0][kt], mfma(C64[st][1], Vr[1][kt], mfma(C64[st][0], Vr[0][kt], Z4))));
                const v4f ri = mfma(C64[st][1], Vi[1][kt], mfma(C64[st][0], Vi[0][kt], mfma(S64[st][1], Vr[1][kt], mfma(S64[st][0], Vr[0][kt], Z4))));
                v4f tr, ti;
#pragma unroll
                for (int r = 0; r < 4; ++r) { tr[r] = rr[r] * twc[st][kt][r] - ri[r] * tws[st][kt][r]; ti[r] = ri[r] * twc[st][kt][r] + rr[r] * tws[st][kt][r]; }
                const unsigned ad = ltr + (16 * kt + l15) * 136 + (16 * st + 4 * g) * 2;
                *(LAS v2u*)ad = (v2u){cvtpk(tr.x, tr.y), cvtpk(tr.z, tr.w)}; *(LAS v2u*)(ad + 4352) = (v2u){cvtpk(ti.x, ti.y), cvtpk(ti.z, ti.w)}; }
        asm volatile("s_waitcnt lgkmcnt(0)" ::: "memory");
#pragma unroll
        for (int m2 = 0; m2 < 2; ++m2) { v4f yr[2][2], yi[2][2];
#pragma unroll
            for (int y = 0; y < 2; ++y) { const unsigned ad = ltr + (8 * g + (l15 >> 2)) * 136 + (32 * m2 + 8 * (lane & 3) + 4 * y) * 2;
                const v2u p = trread(ad), q = trread(ad + 544), r = trread(ad + 4352), s = trread(ad + 4896);
                const v4u xr = {p.x, p.y, q.x, q.y}, xi = {r.x, r.y, s.x, s.y};
#pragma unroll
                for (int kt = 0; kt < 2; ++kt) { yr[y][kt] = mfma(xi, N32[kt], mfma(xr, C32[kt], Z4)); yi[y][kt] = mfma(xi, C32[kt], mfma(xr, S32[kt], Z4)); } }
#pragma unroll
            for (int kt = 0; kt < 2; ++kt) { const unsigned vo = ((16 * kt + l15) * 64 + 32 * m2 + 8 * g) * 2;
                st16(rw, pack8(yr[0][kt], yr[1][kt]), vo, O_Y2 + rre * 4096);
                if (cplx) st16(rw, pack8(yi[0][kt], yi[1][kt]), vo, O_Y2 + (rre + 1) * 4096); } }
        asm volatile("s_waitcnt lgkmcnt(0)" ::: "memory");
    }
}
DI void fft_role(rsrc_t rw, Tag tag, int fi, unsigned ldsb) {
    const Args a = get_args();
    const int lane = fresh_lane(), w = RFL(threadIdx.x >> 6), tid = w * 64 + lane, l15 = lane & 15, g = lane >> 4;
    {
        const rsrc_t rx = __builtin_amdgcn_make_buffer_rsrc((void*)a.ax, 0, 2048 * 896, 0x00020000);
        dma_rows<true>(rx, 2 * fi, ldsb, w, lane); dma_rows<true>(rx, 2 * fi + 1, ldsb + SEGH, w, lane);
        wait_flags(rw, tag, F_T, 64, 64, lane);
        wait_flags(rw, tag, F_MH + FFTW * 64, 0, 1, lane, true);
        Wreg W; load_w<false>(W, rw, 32, w, lane);
        WAIT_VM(0); BAR();
        for (int k = 0; k < 2; ++k) { v4f c[2][7]; gemm2<false, 3>(c, xaddr<true>(ldsb + k * SEGH, lane), W);
#pragma unroll
            for (int t = 0; t < 7; ++t) st16(rw, pack8(c[0][t], c[1][t]), l15 * 4096 + g * 16, O_Y1 + (7 * w + t) * 65536 + (2 * fi + k) * 64); }
    }
    publish(rw, tag, F_F1 + fi * 64);
    wait_flags(rw, tag, F_F1, 64, 32, lane);
    fft_columns(rw, fi, ldsb);
    publish(rw, tag, F_F2 + fi * 64);
    {
        const rsrc_t ro = __builtin_amdgcn_make_buffer_rsrc(get_args().out, 0, 2048 * 1792, 0x00020000);
        Wreg W; load_w<true>(W, rw, 33, w, lane);
        wait_flags(rw, tag, F_F2, 64, 32, lane);
#pragma unroll
        for (int k = 0; k < 14; ++k) { const int id = w * 14 + k, sg = id / 28, blk = id % 28;
            dma16(rw, (lane >> 2) * 4096 + (lane & 3) * 16, O_Y2 + blk * 65536 + (2 * fi + sg) * 64, ldsb + id * 1024); }
        WAIT_VM(0); BAR();
        for (int k = 0; k < 2; ++k) { v4f c[2][7]; gemm2<true, 1>(c, ldsb + k * SEGH + (8 * g + (l15 >> 2)) * 64 + (lane & 3) * 8, W);
#pragma unroll
            for (int jt = 0; jt < 2; ++jt) { const unsigned o = l15 * 1792 + 16 * g, so = ((2 * fi + k) * 32 + 16 * jt) * 1792;
#pragma unroll
                for (int b = 0; b < 3; ++b) st16(ro, pack8(c[jt][2 * b], c[jt][2 * b + 1]), o, so + 64 * (3 * w + b));
                st8(ro, (v2u){cvtpk(c[jt][6].x, c[jt][6].y), cvtpk(c[jt][6].z, c[jt][6].w)}, o, so + 64 * (12 + (w >> 1)) + 8 * (w & 1)); } }
    }
}

#define CHAIN_ASM \
    "v_mbcnt_lo_u32_b32 v112, -1, 0\n" \
    "v_mbcnt_hi_u32_b32 v112, -1, v112\n" \
    "v_lshlrev_b32 v112, 2, v112\n" \
    "v_add_u32 v112, %1, v112\n" \
    "ds_read_b32 v0, v112 offset:0\n" \
    "ds_read_b32 v1, v112 offset:1024\n" \
    "ds_read_b32 v2, v112 offset:2048\n" \
    "ds_read_b32 v3, v112 offset:3072\n" \
    "ds_read_b32 v4, v112 offset:4096\n" \
    "ds_read_b32 v5, v112 offset:5120\n" \
    "ds_read_b32 v6, v112 offset:6144\n" \
    "ds_read_b32 v7, v112 offset:7168\n" \
    "ds_read_b32 v8, v112 offset:8192\n" \
    "ds_read_b32 v9, v112 offset:9216\n" \
    "ds_read_b32 v10, v112 offset:10240\n" \
    "ds_read_b32 v11, v112 offset:11264\n" \
    "ds_read_b32 v12, v112 offset:12288\n" \
    "ds_read_b32 v13, v112 offset:13312\n" \
    "ds_read_b32 v14, v112 offset:14336\n" \
    "ds_read_b32 v15, v112 offset:15360\n" \
    "ds_read_b32 v16, v112 offset:16384\n" \
    "ds_read_b32 v17, v112 offset:17408\n" \
    "ds_read_b32 v18, v112 offset:18432\n" \
    "ds_read_b32 v19, v112 offset:19456\n" \
    "ds_read_b32 v22, v112 offset:20480\n" \
    "ds_read_b32 v23, v112 offset:21504\n" \
    "ds_read_b32 v24, v112 offset:22528\n" \
    "ds_read_b32 v25, v112 offset:23552\n" \
    "ds_read_b32 v26, v112 offset:24576\n" \
    "ds_read_b32 v27, v112 offset:25600\n" \
    "ds_read_b32 v28, v112 offset:26624\n" \
    "ds_read_b32 v29, v112 offset:27648\n" \
    "ds_read_b32 v114, v112 offset:28672\n" \
    "ds_read_b32 v115, v112 offset:29696\n" \
    "v_mov_b32 v20, -1\n" \
    "s_waitcnt lgkmcnt(0)\n" \
    "s_mov_b32 s40, %5\n" \
    "buffer_load_dwordx4 a[168:171], v114, %0, s40 offen sc1\n" \
    "buffer_load_dwordx4 a[172:175], v114, %0, s40 offen offset:1024 sc1\n" \
    "s_add_u32 s40, s40, 2048\n" \
    "buffer_load_dwordx4 a[176:179], v114, %0, s40 offen sc1\n" \
    "buffer_load_dwordx4 a[180:183], v114, %0, s40 offen offset:1024 sc1\n" \
    "s_add_u32 s40, s40, 2048\n" \
    "buffer_load_dwordx4 a[184:187], v114, %0, s40 offen sc1\n" \
    "buffer_load_dwordx4 a[188:191], v114, %0, s40 offen offset:1024 sc1\n" \
    "s_add_u32 s40, s40, 2048\n" \
    "buffer_load_dwordx4 a[192:195], v114, %0, s40 offen sc1\n" \
    "buffer_load_dwordx4 a[196:199], v114, %0, s40 offen offset:1024 sc1\n" \
    "s_add_u32 s40, s40, 2048\n" \
    "buffer_load_dwordx4 a[200:203], v114, %0, s40 offen sc1\n" \
    "buffer_load_dwordx4 a[204:207], v114, %0, s40 offen offset:1024 sc1\n" \
    "s_add_u32 s40, s40, 2048\n" \
    "buffer_load_dwordx4 a[208:211], v114, %0, s40 offen sc1\n" \
    "buffer_load_dwordx4 a[212:215], v114, %0, s40 offen offset:1024 sc1\n" \
    "s_add_u32 s40, s40, 2048\n" \
    "buffer_load_dwordx4 a[216:219], v114, %0, s40 offen sc1\n" \
    "buffer_load_dwordx4 a[220:223], v114, %0, s40 offen offset:1024 sc1\n" \
    "s_add_u32 s40, s40, 2048\n" \
    "buffer_load_dwordx4 a[224:227], v114, %0, s40 offen sc1\n" \
    "buffer_load_dwordx4 a[228:231], v114, %0, s40 offen offset:1024 sc1\n" \
    "s_add_u32 s40, s40, 2048\n" \
    "buffer_load_dwordx4 a[232:235], v114, %0, s40 offen sc1\n" \
    "buffer_load_dwordx4 a[236:239], v114, %0, s40 offen offset:1024 sc1\n" \
    "s_add_u32 s40, s40, 2048\n" \
    "buffer_load_dwordx4 a[240:243], v114, %0, s40 offen sc1\n" \
    "buffer_load_dwordx4 a[244:247], v114, %0, s40 offen offset:1024 sc1\n" \
    "s_add_u32 s40, s40, 2048\n" \
    "buffer_load_dwordx4 a[248:251], v114, %0, s40 offen sc1\n" \
    "buffer_load_dwordx4 a[252:255], v114, %0, s40 offen offset:1024 sc1\n" \
    "s_add_u32 s40, s40, 2048\n" \
    "buffer_load_dwordx4 v[144:147], v114, %0, s40 offen sc1\n" \
    "buffer_load_dwordx4 v[148:151], v114, %0, s40 offen offset:1024 sc1\n" \
    "s_add_u32 s40, s40, 2048\n" \
    "buffer_load_dwordx4 v[152:155], v114, %0, s40 offen sc1\n" \
    "buffer_load_dwordx4 v[156:159], v114, %0, s40 offen offset:1024 sc1\n" \
    "s_add_u32 s40, s40, 2048\n" \
    "buffer_load_dwordx4 v[160:163], v114, %0, s40 offen sc1\n" \
    "buffer_load_dwordx4 v[164:167], v114, %0, s40 offen offset:1024 sc1\n" \
    "s_add_u32 s40, s40, 2048\n" \
    "buffer_load_dwordx4 v[168:171], v114, %0, s40 offen sc1\n" \
    "buffer_load_dwordx4 v[172:175], v114, %0, s40 offen offset:1024 sc1\n" \
    "s_add_u32 s40, s40, 2048\n" \
    "buffer_load_dwordx4 v[176:179], v114, %0, s40 offen sc1\n" \
    "buffer_load_dwordx4 v[180:183], v114, %0, s40 offen offset:1024 sc1\n" \
    "s_add_u32 s40, s40, 2048\n" \
    "buffer_load_dwordx4 v[184:187], v114, %0, s40 offen sc1\n" \
    "buffer_load_dwordx4 v[188:191], v114, %0, s40 offen offset:1024 sc1\n" \
    "s_add_u32 s40, s40, 2048\n" \
    "buffer_load_dwordx4 v[192:195], v114, %0, s40 offen sc1\n" \
    "buffer_load_dwordx4 v[196:199], v114, %0, s40 offen offset:1024 sc1\n" \
    "s_add_u32 s40, s40, 2048\n" \
    "buffer_load_dwordx4 v[200:203], v114, %0, s40 offen sc1\n" \
    "buffer_load_dwordx4 v[204:207], v114, %0, s40 offen offset:1024 sc1\n" \
    "s_add_u32 s40, s40, 2048\n" \
    "buffer_load_dwordx4 v[208:211], v114, %0, s40 offen sc1\n" \
    "buffer_load_dwordx4 v[212:215], v114, %0, s40 offen offset:1024 sc1\n" \
    "s_add_u32 s40, s40, 2048\n" \
    "buffer_load_dwordx4 v[216:219], v114, %0, s40 offen sc1\n" \
    "buffer_load_dwordx4 v[220:223], v114, %0, s40 offen offset:1024 sc1\n" \
    "s_add_u32 s40, s40, 2048\n" \
    "s_mov_b32 s40, %6\n" \
    "buffer_load_dwordx4 v[224:227], v114, %0, s40 offen sc1\n" \
    "buffer_load_dwordx4 v[228:231], v114, %0, s40 offen offset:1024 sc1\n" \
    "s_add_u32 s40, s40, 2048\n" \
    "buffer_load_dwordx4 v[232:235], v114, %0, s40 offen sc1\n" \
    "buffer_load_dwordx4 v[236:239], v114, %0, s40 offen offset:1024 sc1\n" \
    "s_add_u32 s40, s40, 2048\n" \
    "buffer_load_dwordx4 v[240:243], v114, %0, s40 offen sc1\n" \
    "buffer_load_dwordx4 v[244:247], v114, %0, s40 offen offset:1024 sc1\n" \
    "s_add_u32 s40, s40, 2048\n" \
    "buffer_load_dwordx4 v[248:251], v114, %0, s40 offen sc1\n" \
    "buffer_load_dwordx4 v[252:255], v114, %0, s40 offen offset:1024 sc1\n" \
    "s_add_u32 s40, s40, 2048\n" \
    "s_mov_b32 s46, 0\n" \
    "CH_WF%=:\n" \
    "s_sleep 2\n" \
    "buffer_load_dwordx2 v[116:117], v115, %0, 0 offen sc1\n" \
    "s_add_u32 s46, s46, 1\n" \
    "s_cmp_ge_u32 s46, 262144\n" \
    "s_cbranch_scc1 CH_DEAD%=\n" \
    "s_waitcnt vmcnt(0)\n" \
    "v_cmp_ne_u32 vcc, %13, v116\n" \
    "v_cmp_ne_u32 s[66:67], %14, v117\n" \
    "s_or_b64 vcc, vcc, s[66:67]\n" \
    "s_cbranch_scc1 CH_WF%=\n" \
    "s_barrier\n" \
    "s_add_u32 s40, %7, 0\n" \
    "buffer_load_dwordx4 a[0:3], v114, %0, s40 offen sc1\n" \
    "buffer_load_dwordx4 a[4:7], v114, %0, s40 offen offset:1024 sc1\n" \
    "s_add_u32 s40, s40, 2048\n" \
    "buffer_load_dwordx4 a[8:11], v114, %0, s40 offen sc1\n" \
    "buffer_load_dwordx4 a[12:15], v114, %0, s40 offen offset:1024 sc1\n" \
    "s_add_u32 s40, s40, 2048\n" \
    "buffer_load_dwordx4 a[16:19], v114, %0, s40 offen sc1\n" \
    "buffer_load_dwordx4 a[20:23], v114, %0, s40 offen offset:1024 sc1\n" \
    "s_add_u32 s40, s40, 2048\n" \
    "buffer_load_dwordx4 a[24:27], v114, %0, s40 offen sc1\n" \
    "buffer_load_dwordx4 a[28:31], v114, %0, s40 offen offset:1024 sc1\n" \
    "s_add_u32 s40, s40, 2048\n" \
    "buffer_load_dwordx4 a[32:35], v114, %0, s40 offen sc1\n" \
    "buffer_load_dwordx4 a[36:39], v114, %0, s40 offen offset:1024 sc1\n" \
    "s_add_u32 s40, s40, 2048\n" \
    "buffer_load_dwordx4 a[40:43], v114, %0, s40 offen sc1\n" \
    "buffer_load_dwordx4 a[44:47], v114, %0, s40 offen offset:1024 sc1\n" \
    "s_add_u32 s40, s40, 2048\n" \
    "buffer_load_dwordx4 a[48:51], v114, %0, s40 offen sc1\n" \
    "buffer_load_dwordx4 a[52:55], v114, %0, s40 offen offset:1024 sc1\n" \
    "s_add_u32 s40, s40, 2048\n" \
    "s_waitcnt vmcnt(30)\n" \
    "s_add_u32 s40, %7, 802816\n" \
    "buffer_load_dwordx4 a[56:59], v114, %0, s40 offen sc1\n" \
    "buffer_load_dwordx4 a[60:63], v114, %0, s40 offen offset:1024 sc1\n" \
    "s_add_u32 s40, s40, 2048\n" \
    "buffer_load_dwordx4 a[64:67], v114, %0, s40 offen sc1\n" \
    "buffer_load_dwordx4 a[68:71], v114, %0, s40 offen offset:1024 sc1\n" \
    "s_add_u32 s40, s40, 2048\n" \
    "buffer_load_dwordx4 a[72:75], v114, %0, s40 offen sc1\n" \
    "buffer_load_dwordx4 a[76:79], v114, %0, s40 offen offset:1024 sc1\n" \
    "s_add_u32 s40, s40, 2048\n" \
    "buffer_load_dwordx4 a[80:83], v114, %0, s40 offen sc1\n" \
    "buffer_load_dwordx4 a[84:87], v114, %0, s40 offen offset:1024 sc1\n" \
    "s_add_u32 s40, s40, 2048\n" \
    "buffer_load_dwordx4 a[88:91], v114, %0, s40 offen sc1\n" \
    "buffer_load_dwordx4 a[92:95], v114, %0, s40 offen offset:1024 sc1\n" \
    "s_add_u32 s40, s40, 2048\n" \
    "buffer_load_dwordx4 a[96:99], v114, %0, s40 offen sc1\n" \
    "buffer_load_dwordx4 a[100:103], v114, %0, s40 offen offset:1024 sc1\n" \
    "s_add_u32 s40, s40, 2048\n" \
    "buffer_load_dwordx4 a[104:107], v114, %0, s40 offen sc1\n" \
    "buffer_load_dwordx4 a[108:111], v114, %0, s40 offen offset:1024 sc1\n" \
    "s_add_u32 s40, s40, 2048\n" \
    "s_waitcnt vmcnt(30)\n" \
    "s_add_u32 s40, %7, 1605632\n" \
    "buffer_load_dwordx4 a[112:115], v114, %0, s40 offen sc1\n" \
    "buffer_load_dwordx4 a[116:119], v114, %0, s40 offen offset:1024 sc1\n" \
    "s_add_u32 s40, s40, 2048\n" \
    "buffer_load_dwordx4 a[120:123], v114, %0, s40 offen sc1\n" \
    "buffer_load_dwordx4 a[124:127], v114, %0, s40 offen offset:1024 sc1\n" \
    "s_add_u32 s40, s40, 2048\n" \
    "buffer_load_dwordx4 a[128:131], v114, %0, s40 offen sc1\n" \
    "buffer_load_dwordx4 a[132:135], v114, %0, s40 offen offset:1024 sc1\n" \
    "s_add_u32 s40, s40, 2048\n" \
    "buffer_load_dwordx4 a[136:139], v114, %0, s40 offen sc1\n" \
    "buffer_load_dwordx4 a[140:143], v114, %0, s40 offen offset:1024 sc1\n" \
    "s_add_u32 s40, s40, 2048\n" \
    "buffer_load_dwordx4 a[144:147], v114, %0, s40 offen sc1\n" \
    "buffer_load_dwordx4 a[148:151], v114, %0, s40 offen offset:1024 sc1\n" \
    "s_add_u32 s40, s40, 2048\n" \
    "buffer_load_dwordx4 a[152:155], v114, %0, s40 offen sc1\n" \
    "buffer_load_dwordx4 a[156:159], v114, %0, s40 offen offset:1024 sc1\n" \
    "s_add_u32 s40, s40, 2048\n" \
    "buffer_load_dwordx4 a[160:163], v114, %0, s40 offen sc1\n" \
    "buffer_load_dwordx4 a[164:167], v114, %0, s40 offen offset:1024 sc1\n" \
    "s_add_u32 s40, s40, 2048\n" \
    "s_waitcnt vmcnt(30)\n" \
    "s_add_u32 s40, %7, 2408448\n" \
    "buffer_load_dwordx4 v[32:35], v114, %0, s40 offen sc1\n" \
    "s_add_u32 s40, s40, 1024\n" \
    "buffer_load_dwordx4 v[36:39], v114, %0, s40 offen sc1\n" \
    "s_add_u32 s40, s40, 1024\n" \
    "buffer_load_dwordx4 v[40:43], v114, %0, s40 offen sc1\n" \
    "s_add_u32 s40, s40, 1024\n" \
    "buffer_load_dwordx4 v[44:47], v114, %0, s40 offen sc1\n" \
    "s_add_u32 s40, s40, 1024\n" \
    "buffer_load_dwordx4 v[48:51], v114, %0, s40 offen sc1\n" \
    "s_add_u32 s40, s40, 1024\n" \
    "buffer_load_dwordx4 v[52:55], v114, %0, s40 offen sc1\n" \
    "s_add_u32 s40, s40, 1024\n" \
    "buffer_load_dwordx4 v[56:59], v114, %0, s40 offen sc1\n" \
    "s_add_u32 s40, s40, 1024\n" \
    "buffer_load_dwordx4 v[60:63], v114, %0, s40 offen sc1\n" \
    "s_add_u32 s40, s40, 1024\n" \
    "buffer_load_dwordx4 v[64:67], v114, %0, s40 offen sc1\n" \
    "s_add_u32 s40, s40, 1024\n" \
    "buffer_load_dwordx4 v[68:71], v114, %0, s40 offen sc1\n" \
    "s_add_u32 s40, s40, 1024\n" \
    "buffer_load_dwordx4 v[72:75], v114, %0, s40 offen sc1\n" \
    "s_add_u32 s40, s40, 1024\n" \
    "buffer_load_dwordx4 v[76:79], v114, %0, s40 offen sc1\n" \
    "s_add_u32 s40, s40, 1024\n" \
    "buffer_load_dwordx4 v[80:83], v114, %0, s40 offen sc1\n" \
    "s_add_u32 s40, s40, 1024\n" \
    "buffer_load_dwordx4 v[84:87], v114, %0, s40 offen sc1\n" \
    "s_add_u32 s40, s40, 1024\n" \
    "s_waitcnt vmcnt(0)\n" \
    "ds_write_b128 v11, v[32:35] offset:0\n" \
    "ds_write_b128 v11, v[36:39] offset:1024\n" \
    "ds_write_b128 v11, v[40:43] offset:2048\n" \
    "ds_write_b128 v11, v[44:47] offset:3072\n" \
    "ds_write_b128 v11, v[48:51] offset:4096\n" \
    "ds_write_b128 v11, v[52:55] offset:5120\n" \
    "ds_write_b128 v11, v[56:59] offset:6144\n" \
    "ds_write_b128 v11, v[60:63] offset:7168\n" \
    "ds_write_b128 v11, v[64:67] offset:8192\n" \
    "ds_write_b128 v11, v[68:71] offset:9216\n" \
    "ds_write_b128 v11, v[72:75] offset:10240\n" \
    "ds_write_b128 v11, v[76:79] offset:11264\n" \
    "ds_write_b128 v11, v[80:83] offset:12288\n" \
    "ds_write_b128 v11, v[84:87] offset:13312\n" \
    "s_and_b32 s40, %2, 1\n" \
    "s_cmp_eq_u32 s40, 0\n" \
    "s_cbranch_scc1 CH_NOZ%=\n" \
    "v_mov_b64 v[248:249], 0\n" \
    "v_mov_b64 v[250:251], 0\n" \
    "v_mov_b64 v[252:253], 0\n" \
    "v_mov_b64 v[254:255], 0\n" \
    "CH_NOZ%=:\n" \
    "s_waitcnt lgkmcnt(0)\n" \
    "s_mov_b32 s40, 0\n" \
    "s_mov_b32 s46, 0\n" \
    "s_mov_b32 s54, 0\n" \
    "s_mov_b32 s55, 0\n" \
    "s_mov_b32 s41, %3\n" \
    "s_add_u32 s42, s41, 131072\n" \
    "s_add_u32 s44, %4, 524288\n" \
    "v_mov_b64 v[80:81], 0\n" \
    "v_mov_b64 v[82:83], 0\n" \
    "v_mov_b64 v[96:97], 0\n" \
    "v_mov_b64 v[98:99], 0\n" \
    "v_mov_b64 v[84:85], 0\n" \
    "v_mov_b64 v[86:87], 0\n" \
    "v_mov_b64 v[100:101], 0\n" \
    "v_mov_b64 v[102:103], 0\n" \
    "v_mov_b64 v[88:89], 0\n" \
    "v_mov_b64 v[90:91], 0\n" \
    "v_mov_b64 v[104:105], 0\n" \
    "v_mov_b64 v[106:107], 0\n" \
    "v_mov_b64 v[92:93], 0\n" \
    "v_mov_b64 v[94:95], 0\n" \
    "v_mov_b64 v[108:109], 0\n" \
    "v_mov_b64 v[110:111], 0\n" \
    "v_mov_b32 v30, 0\n" \
    "ds_write_b32 v18, v30\n" \
    "s_mov_b32 s70, 1\n" \
    "s_waitcnt lgkmcnt(0)\n" \
    "s_memrealtime s[58:59]\n" \
    "s_waitcnt lgkmcnt(0)\n" \
    "s_sub_u32 s58, s58, %15\n" \
    "s_mul_hi_u32 s58, s58, 76695845\n" \
    "s_add_u32 s58, s58, 1\n" \
    "s_mul_i32 s58, s58, 56\n" \
    "s_add_u32 s70, s58, %15\n" \
    "s_mov_b32 s80, 0x21800000\n" \
    "s_mov_b32 s81, 0x5d800000\n" \
    "s_mov_b32 s64, 0x00ff00ff\n" \
    "s_mov_b32 s65, 0xff00ff00\n" \
    "s_mov_b32 s68, 0x00ff00ff\n" \
    "s_mov_b32 s69, 0x00ff00ff\n" \
    "s_setprio 3\n" \
    "CH_POLL%=:\n" \
    "s_cmp_eq_u32 %2, 0\n" \
    "s_cbranch_scc1 CH_NDa%=\n" \
    "CH_Wm%=:\n" \
    "s_memrealtime s[58:59]\n" \
    "s_waitcnt lgkmcnt(0)\n" \
    "s_sub_u32 s58, s58, s70\n" \
    "s_cmp_lt_i32 s58, 0\n" \
    "s_cbranch_scc1 CH_Wm%=\n" \
    "s_cmp_gt_i32 s58, 8\n" \
    "s_cbranch_scc1 CH_Xm%=\n" \
    "s_nop 9\n" \
    "s_add_u32 m0, %8, 0\n" \
    "s_cmp_lg_u32 %2, 1\n" \
    "buffer_load_dwordx4 v0, %0, s41 offen sc1 lds\n" \
    "buffer_load_dwordx4 v0, %0, s41 offen offset:1024 sc1 lds\n" \
    "s_cbranch_scc1 CH_NEa%=\n" \
    "s_add_u32 m0, %9, 0\n" \
    "s_nop 0\n" \
    "buffer_load_dwordx4 v1, %0, s41 offen sc1 lds\n" \
    "CH_NEa%=:\n" \
    "s_waitcnt vmcnt(0)\n" \
    "CH_NDa%=:\n" \
    "s_add_u32 s70, s70, 80\n" \
    "s_barrier\n" \
    "CH_QK%=:\n" \
    "ds_read_b128 v[32:35], v2 offset:0\n" \
    "ds_read_b128 v[36:39], v2 offset:1024\n" \
    "ds_read_b128 v[40:43], v2 offset:2048\n" \
    "s_waitcnt lgkmcnt(2)\n" \
    "v_smfmac_f32_16x16x64_bf16 v[80:83], v[32:35], a[168:175], v17\n" \
    "ds_read_b128 v[44:47], v2 offset:3072\n" \
    "v_smfmac_f32_16x16x64_bf16 v[84:87], v[32:35], a[224:231], v17\n" \
    "ds_read_b128 v[48:51], v2 offset:4096\n" \
    "s_waitcnt lgkmcnt(3)\n" \
    "v_smfmac_f32_16x16x64_bf16 v[80:83], v[36:39], a[176:183], v17\n" \
    "ds_read_b128 v[52:55], v2 offset:5120\n" \
    "v_smfmac_f32_16x16x64_bf16 v[84:87], v[36:39], a[232:239], v17\n" \
    "ds_read_b128 v[56:59], v2 offset:6144\n" \
    "s_waitcnt lgkmcnt(4)\n" \
    "v_smfmac_f32_16x16x64_bf16 v[80:83], v[40:43], a[184:191], v17\n" \
    "ds_read_b128 v[60:63], v3 offset:0\n" \
    "v_smfmac_f32_16x16x64_bf16 v[84:87], v[40:43], a[240:247], v17\n" \
    "ds_read_b128 v[64:67], v3 offset:1024\n" \
    "s_waitcnt lgkmcnt(5)\n" \
    "v_smfmac_f32_16x16x64_bf16 v[80:83], v[44:47], a[192:199], v17\n" \
    "ds_read_b128 v[68:71], v3 offset:2048\n" \
    "v_smfmac_f32_16x16x64_bf16 v[84:87], v[44:47], a[248:255], v17\n" \
    "ds_read_b128 v[72:75], v4 offset:3072\n" \
    "s_waitcnt lgkmcnt(6)\n" \
    "v_smfmac_f32_16x16x64_bf16 v[80:83], v[48:51], a[200:207], v17\n" \
    "ds_read2_b32 v[76:77], v5 offset0:0 offset1:32\n" \
    "v_smfmac_f32_16x16x64_bf16 v[84:87], v[48:51], v[144:151], v17\n" \
    "ds_read2_b32 v[78:79], v6 offset0:0 offset1:32\n" \
    "s_waitcnt lgkmcnt(7)\n" \
    "v_smfmac_f32_16x16x64_bf16 v[80:83], v[52:55], a[208:215], v17\n" \
    "v_smfmac_f32_16x16x64_bf16 v[84:87], v[52:55], v[152:159], v17\n" \
    "s_waitcnt lgkmcnt(6)\n" \
    "v_smfmac_f32_16x16x64_bf16 v[80:83], v[56:59], a[216:223], v17\n" \
    "v_smfmac_f32_16x16x64_bf16 v[84:87], v[56:59], v[160:167], v17\n" \
    "s_waitcnt lgkmcnt(0)\n" \
    "v_smfmac_f32_16x16x64_bf16 v[88:91], v[32:35], v[168:175], v17\n" \
    "v_smfmac_f32_16x16x64_bf16 v[92:95], v[60:63], v[224:231], v17\n" \
    "v_smfmac_f32_16x16x64_bf16 v[88:91], v[36:39], v[176:183], v17\n" \
    "v_smfmac_f32_16x16x64_bf16 v[92:95], v[64:67], v[232:239], v17\n" \
    "v_smfmac_f32_16x16x64_bf16 v[88:91], v[40:43], v[184:191], v17\n" \
    "v_smfmac_f32_16x16x64_bf16 v[92:95], v[68:71], v[240:247], v17\n" \
    "v_smfmac_f32_16x16x64_bf16 v[88:91], v[44:47], v[192:199], v17\n" \
    "v_smfmac_f32_16x16x64_bf16 v[92:95], v[72:75], v[248:255], v17\n" \
    "v_smfmac_f32_16x16x64_bf16 v[88:91], v[48:51], v[200:207], v17\n" \
    "v_cvt_pk_bf16_f32 v112, v80, v81\n" \
    "v_cvt_pk_bf16_f32 v113, v82, v83\n" \
    "v_cvt_pk_bf16_f32 v114, v84, v85\n" \
    "v_cvt_pk_bf16_f32 v115, v86, v87\n" \
    "v_smfmac_f32_16x16x64_bf16 v[88:91], v[52:55], v[208:215], v17\n" \
    "s_nop 0\n" \
    "v_mfma_f32_16x16x32_bf16 v[120:123], v[112:115], v[22:25], 0\n" \
    "v_smfmac_f32_16x16x64_bf16 v[88:91], v[56:59], v[216:223], v17\n" \
    "v_cvt_pk_bf16_f32 v118, v92, v93\n" \
    "v_cvt_pk_bf16_f32 v119, v94, v95\n" \
    "s_nop 4\n" \
    "v_cvt_pk_bf16_f32 v128, v120, v121\n" \
    "v_cvt_pk_bf16_f32 v116, v88, v89\n" \
    "v_cvt_pk_bf16_f32 v117, v90, v91\n" \
    "v_cvt_pk_bf16_f32 v129, v122, v123\n" \
    "s_nop 0\n" \
    "v_mfma_f32_16x16x32_bf16 v[124:127], v[116:119], v[22:25], 0\n" \
    "s_nop 7\n" \
    "v_cvt_pk_bf16_f32 v130, v124, v125\n" \
    "v_cvt_pk_bf16_f32 v131, v126, v127\n" \
    "s_nop 1\n" \
    "v_mfma_f32_16x16x32_bf16 v[120:123], v[128:131], v[76:79], 0\n" \
    "s_nop 4\n" \
    "s_mov_b64 exec, s[64:65]\n" \
    "s_nop 1\n" \
    "ds_write_b128 v7, v[120:123]\n" \
    "v_cmp_u_f32 vcc, v120, v121\n" \
    "v_cmp_u_f32 s[66:67], v122, v123\n" \
    "s_mov_b64 exec, -1\n" \
    "s_or_b64 vcc, vcc, s[66:67]\n" \
    "s_cbranch_scc1 CH_CHKM%=\n" \
    "CH_B2%=:\n" \
    "s_cmp_lg_u32 %2, 0\n" \
    "s_cbranch_scc1 CH_U1%=\n" \
    "s_waitcnt lgkmcnt(0)\n" \
    "s_barrier\n" \
    "ds_read2st64_b32 v[112:113], v8 offset0:0 offset1:1\n" \
    "ds_read2st64_b32 v[114:115], v8 offset0:2 offset1:3\n" \
    "ds_read2st64_b32 v[116:117], v8 offset0:4 offset1:5\n" \
    "ds_read2st64_b32 v[118:119], v8 offset0:6 offset1:7\n" \
    "s_waitcnt lgkmcnt(2)\n" \
    "v_add_f32 v112, v112, v113\n" \
    "v_add_f32 v114, v114, v115\n" \
    "s_waitcnt lgkmcnt(0)\n" \
    "v_add_f32 v116, v116, v117\n" \
    "v_add_f32 v118, v118, v119\n" \
    "v_add_f32 v112, v112, v114\n" \
    "v_add_f32 v116, v116, v118\n" \
    "v_add_f32 v112, v112, v116\n" \
    "buffer_store_dword v112, v12, %0, s42 offen sc1\n" \
    "v_smfmac_f32_16x16x64_bf16 v[96:99], v[32:35], a[0:7], v17\n" \
    "s_add_u32 s45, s40, 1\n" \
    "v_smfmac_f32_16x16x64_bf16 v[104:107], v[32:35], a[112:119], v17\n" \
    "s_and_b32 s47, s45, 3\n" \
    "v_smfmac_f32_16x16x64_bf16 v[96:99], v[36:39], a[8:15], v17\n" \
    "s_lshl_b32 s50, s47, 15\n" \
    "v_smfmac_f32_16x16x64_bf16 v[104:107], v[36:39], a[120:127], v17\n" \
    "s_mul_i32 s51, s47, 229376\n" \
    "v_smfmac_f32_16x16x64_bf16 v[96:99], v[40:43], a[16:23], v17\n" \
    "s_add_u32 s47, s50, %3\n" \
    "v_smfmac_f32_16x16x64_bf16 v[104:107], v[40:43], a[128:135], v17\n" \
    "s_add_u32 s48, s40, 3\n" \
    "ds_read_b128 v[60:63], v11 offset:0\n" \
    "ds_read_b128 v[64:67], v11 offset:1024\n" \
    "v_smfmac_f32_16x16x64_bf16 v[96:99], v[44:47], a[24:31], v17\n" \
    "s_and_b32 s48, s48, 3\n" \
    "v_smfmac_f32_16x16x64_bf16 v[104:107], v[44:47], a[136:143], v17\n" \
    "s_lshl_b32 s48, s48, 15\n" \
    "ds_read_b128 v[68:71], v11 offset:2048\n" \
    "ds_read_b128 v[72:75], v11 offset:3072\n" \
    "v_smfmac_f32_16x16x64_bf16 v[96:99], v[48:51], a[32:39], v17\n" \
    "s_add_u32 s48, s48, %3\n" \
    "CH_Wsa%=:\n" \
    "s_memrealtime s[58:59]\n" \
    "s_waitcnt lgkmcnt(0)\n" \
    "s_sub_u32 s58, s58, s70\n" \
    "s_cmp_lt_i32 s58, 0\n" \
    "s_cbranch_scc1 CH_Wsa%=\n" \
    "s_cmp_gt_i32 s58, 8\n" \
    "s_cbranch_scc1 CH_Xsa%=\n" \
    "s_add_u32 s70, s70, 68\n" \
    "buffer_load_dwordx4 v[80:83], v13, %0, s42 offen sc1\n" \
    "buffer_load_dwordx4 v[84:87], v13, %0, s42 offen offset:256 sc1\n" \
    "v_smfmac_f32_16x16x64_bf16 v[104:107], v[48:51], a[144:151], v17\n" \
    "s_lshl_b32 s49, s45, 13\n" \
    "ds_read_b128 v[112:115], v11 offset:4096\n" \
    "ds_read_b128 v[116:119], v11 offset:5120\n" \
    "v_smfmac_f32_16x16x64_bf16 v[96:99], v[52:55], a[40:47], v17\n" \
    "s_add_u32 s49, s49, 1441792\n" \
    "v_smfmac_f32_16x16x64_bf16 v[104:107], v[52:55], a[152:159], v17\n" \
    "ds_read_b128 v[120:123], v11 offset:6144\n" \
    "ds_read_b128 v[124:127], v11 offset:7168\n" \
    "v_smfmac_f32_16x16x64_bf16 v[96:99], v[56:59], a[48:55], v17\n" \
    "v_smfmac_f32_16x16x64_bf16 v[104:107], v[56:59], a[160:167], v17\n" \
    "v_smfmac_f32_16x16x64_bf16 v[100:103], v[32:35], a[56:63], v17\n" \
    "s_waitcnt lgkmcnt(6)\n" \
    "v_smfmac_f32_16x16x64_bf16 v[108:111], v[32:35], v[60:67], v17\n" \
    "ds_read_b128 v[60:63], v11 offset:8192\n" \
    "ds_read_b128 v[64:67], v11 offset:9216\n" \
    "v_smfmac_f32_16x16x64_bf16 v[100:103], v[36:39], a[64:71], v17\n" \
    "s_waitcnt lgkmcnt(6)\n" \
    "v_smfmac_f32_16x16x64_bf16 v[108:111], v[36:39], v[68:75], v17\n" \
    "ds_read_b128 v[68:71], v11 offset:10240\n" \
    "ds_read_b128 v[72:75], v11 offset:11264\n" \
    "v_smfmac_f32_16x16x64_bf16 v[100:103], v[40:43], a[72:79], v17\n" \
    "v_permlane32_swap_b32 v96, v104\n" \
    "buffer_load_dwordx2 v[92:93], v14, %0, s41 offen sc1\n" \
    "buffer_load_dwordx4 v[88:91], v15, %0, s44 offen sc1\n" \
    "s_waitcnt lgkmcnt(6)\n" \
    "v_smfmac_f32_16x16x64_bf16 v[108:111], v[40:43], v[112:119], v17\n" \
    "ds_read_b128 v[112:115], v11 offset:12288\n" \
    "ds_read_b128 v[116:119], v11 offset:13312\n" \
    "v_permlane32_swap_b32 v97, v105\n" \
    "v_smfmac_f32_16x16x64_bf16 v[100:103], v[44:47], a[80:87], v17\n" \
    "v_permlane32_swap_b32 v98, v106\n" \
    "s_waitcnt lgkmcnt(6)\n" \
    "v_smfmac_f32_16x16x64_bf16 v[108:111], v[44:47], v[120:127], v17\n" \
    "v_permlane32_swap_b32 v99, v107\n" \
    "v_smfmac_f32_16x16x64_bf16 v[100:103], v[48:51], a[88:95], v17\n" \
    "v_add_f32 v96, v96, v104\n" \
    "s_waitcnt lgkmcnt(4)\n" \
    "v_smfmac_f32_16x16x64_bf16 v[108:111], v[48:51], v[60:67], v17\n" \
    "v_add_f32 v97, v97, v105\n" \
    "v_smfmac_f32_16x16x64_bf16 v[100:103], v[52:55], a[96:103], v17\n" \
    "v_add_f32 v98, v98, v106\n" \
    "s_waitcnt lgkmcnt(2)\n" \
    "v_smfmac_f32_16x16x64_bf16 v[108:111], v[52:55], v[68:75], v17\n" \
    "v_add_f32 v99, v99, v107\n" \
    "v_smfmac_f32_16x16x64_bf16 v[100:103], v[56:59], a[104:111], v17\n" \
    "v_cvt_pk_bf16_f32 v132, v96, v97\n" \
    "s_waitcnt lgkmcnt(0)\n" \
    "v_smfmac_f32_16x16x64_bf16 v[108:111], v[56:59], v[112:119], v17\n" \
    "v_cvt_pk_bf16_f32 v133, v98, v99\n" \
    "s_branch CH_U2%=\n" \
    "CH_U1%=:\n" \
    "s_waitcnt lgkmcnt(0)\n" \
    "s_barrier\n" \
    "s_nop 15\n" \
    "s_nop 3\n" \
    "v_smfmac_f32_16x16x64_bf16 v[96:99], v[32:35], a[0:7], v17\n" \
    "s_add_u32 s45, s40, 1\n" \
    "v_smfmac_f32_16x16x64_bf16 v[104:107], v[32:35], a[112:119], v17\n" \
    "s_and_b32 s47, s45, 3\n" \
    "v_smfmac_f32_16x16x64_bf16 v[96:99], v[36:39], a[8:15], v17\n" \
    "s_lshl_b32 s50, s47, 15\n" \
    "v_smfmac_f32_16x16x64_bf16 v[104:107], v[36:39], a[120:127], v17\n" \
    "s_mul_i32 s51, s47, 229376\n" \
    "v_smfmac_f32_16x16x64_bf16 v[96:99], v[40:43], a[16:23], v17\n" \
    "s_add_u32 s47, s50, %3\n" \
    "v_smfmac_f32_16x16x64_bf16 v[104:107], v[40:43], a[128:135], v17\n" \
    "s_add_u32 s48, s40, 3\n" \
    "ds_read_b128 v[60:63], v11 offset:0\n" \
    "ds_read_b128 v[64:67], v11 offset:1024\n" \
    "v_smfmac_f32_16x16x64_bf16 v[96:99], v[44:47], a[24:31], v17\n" \
    "s_and_b32 s48, s48, 3\n" \
    "v_smfmac_f32_16x16x64_bf16 v[104:107], v[44:47], a[136:143], v17\n" \
    "s_lshl_b32 s48, s48, 15\n" \
    "ds_read_b128 v[68:71], v11 offset:2048\n" \
    "ds_read_b128 v[72:75], v11 offset:3072\n" \
    "v_smfmac_f32_16x16x64_bf16 v[96:99], v[48:51], a[32:39], v17\n" \
    "s_add_u32 s48, s48, %3\n" \
    "v_smfmac_f32_16x16x64_bf16 v[104:107], v[48:51], a[144:151], v17\n" \
    "s_lshl_b32 s49, s45, 13\n" \
    "ds_read_b128 v[112:115], v11 offset:4096\n" \
    "ds_read_b128 v[116:119], v11 offset:5120\n" \
    "v_smfmac_f32_16x16x64_bf16 v[96:99], v[52:55], a[40:47], v17\n" \
    "s_add_u32 s49, s49, 1441792\n" \
    "v_smfmac_f32_16x16x64_bf16 v[104:107], v[52:55], a[152:159], v17\n" \
    "ds_read_b128 v[120:123], v11 offset:6144\n" \
    "ds_read_b128 v[124:127], v11 offset:7168\n" \
    "v_smfmac_f32_16x16x64_bf16 v[96:99], v[56:59], a[48:55], v17\n" \
    "v_smfmac_f32_16x16x64_bf16 v[104:107], v[56:59], a[160:167], v17\n" \
    "CH_Wsb%=:\n" \
    "s_memrealtime s[58:59]\n" \
    "s_waitcnt lgkmcnt(0)\n" \
    "s_sub_u32 s58, s58, s70\n" \
    "s_cmp_lt_i32 s58, 0\n" \
    "s_cbranch_scc1 CH_Wsb%=\n" \
    "s_cmp_gt_i32 s58, 8\n" \
    "s_cbranch_scc1 CH_Xsb%=\n" \
    "s_add_u32 s70, s70, 68\n" \
    "s_cmp_eq_u32 %12, 0\n" \
    "s_cbranch_scc1 CH_TS%=\n" \
    "s_add_u32 s52, s40, 1\n" \
    "v_mov_b32 v136, s70\n" \
    "v_mov_b32 v137, s52\n" \
    "s_xor_b32 s52, %3, 16384\n" \
    "buffer_store_dwordx2 v[136:137], v19, %0, s52 offen sc1\n" \
    "CH_TS%=:\n" \
    "buffer_load_dwordx4 v[80:83], v13, %0, s42 offen sc1\n" \
    "buffer_load_dwordx4 v[84:87], v13, %0, s42 offen offset:256 sc1\n" \
    "v_smfmac_f32_16x16x64_bf16 v[100:103], v[32:35], a[56:63], v17\n" \
    "s_waitcnt lgkmcnt(6)\n" \
    "v_smfmac_f32_16x16x64_bf16 v[108:111], v[32:35], v[60:67], v17\n" \
    "ds_read_b128 v[60:63], v11 offset:8192\n" \
    "ds_read_b128 v[64:67], v11 offset:9216\n" \
    "v_smfmac_f32_16x16x64_bf16 v[100:103], v[36:39], a[64:71], v17\n" \
    "s_waitcnt lgkmcnt(6)\n" \
    "v_smfmac_f32_16x16x64_bf16 v[108:111], v[36:39], v[68:75], v17\n" \
    "ds_read_b128 v[68:71], v11 offset:10240\n" \
    "ds_read_b128 v[72:75], v11 offset:11264\n" \
    "v_smfmac_f32_16x16x64_bf16 v[100:103], v[40:43], a[72:79], v17\n" \
    "v_permlane32_swap_b32 v96, v104\n" \
    "s_waitcnt lgkmcnt(6)\n" \
    "v_smfmac_f32_16x16x64_bf16 v[108:111], v[40:43], v[112:119], v17\n" \
    "ds_read_b128 v[112:115], v11 offset:12288\n" \
    "ds_read_b128 v[116:119], v11 offset:13312\n" \
    "v_permlane32_swap_b32 v97, v105\n" \
    "v_smfmac_f32_16x16x64_bf16 v[100:103], v[44:47], a[80:87], v17\n" \
    "v_permlane32_swap_b32 v98, v106\n" \
    "s_waitcnt lgkmcnt(6)\n" \
    "v_smfmac_f32_16x16x64_bf16 v[108:111], v[44:47], v[120:127], v17\n" \
    "v_permlane32_swap_b32 v99, v107\n" \
    "v_smfmac_f32_16x16x64_bf16 v[100:103], v[48:51], a[88:95], v17\n" \
    "v_add_f32 v96, v96, v104\n" \
    "s_waitcnt lgkmcnt(4)\n" \
    "v_smfmac_f32_16x16x64_bf16 v[108:111], v[48:51], v[60:67], v17\n" \
    "v_add_f32 v97, v97, v105\n" \
    "buffer_load_dwordx2 v[92:93], v14, %0, s41 offen sc1\n" \
    "buffer_load_dwordx4 v[88:91], v15, %0, s44 offen sc1\n" \
    "v_smfmac_f32_16x16x64_bf16 v[100:103], v[52:55], a[96:103], v17\n" \
    "v_add_f32 v98, v98, v106\n" \
    "s_waitcnt lgkmcnt(2)\n" \
    "v_smfmac_f32_16x16x64_bf16 v[108:111], v[52:55], v[68:75], v17\n" \
    "v_add_f32 v99, v99, v107\n" \
    "v_smfmac_f32_16x16x64_bf16 v[100:103], v[56:59], a[104:111], v17\n" \
    "v_cvt_pk_bf16_f32 v132, v96, v97\n" \
    "s_waitcnt lgkmcnt(0)\n" \
    "v_smfmac_f32_16x16x64_bf16 v[108:111], v[56:59], v[112:119], v17\n" \
    "v_cvt_pk_bf16_f32 v133, v98, v99\n" \
    "CH_U2%=:\n" \
    "s_nop 5\n" \
    "s_nop 0\n" \
    "v_permlane32_swap_b32 v100, v108\n" \
    "v_permlane32_swap_b32 v101, v109\n" \
    "v_permlane32_swap_b32 v102, v110\n" \
    "v_permlane32_swap_b32 v103, v111\n" \
    "v_add_f32 v100, v100, v108\n" \
    "v_add_f32 v101, v101, v109\n" \
    "v_add_f32 v102, v102, v110\n" \
    "v_add_f32 v103, v103, v111\n" \
    "v_cvt_pk_bf16_f32 v134, v100, v101\n" \
    "v_cvt_pk_bf16_f32 v135, v102, v103\n" \
    "s_nop 0\n" \
    "v_permlane16_swap_b32 v132, v134\n" \
    "v_permlane16_swap_b32 v133, v135\n" \
    "CH_VST%=:\n" \
    "s_waitcnt vmcnt(2)\n" \
    "v_pk_add_f32 v[112:113], v[80:81], v[84:85]\n" \
    "v_pk_add_f32 v[114:115], v[82:83], v[86:87]\n" \
    "v_exp_f32 v112, v112\n" \
    "v_exp_f32 v113, v113\n" \
    "v_exp_f32 v114, v114\n" \
    "v_exp_f32 v115, v115\n" \
    "s_nop 0\n" \
    "v_pk_add_f32 v[118:119], v[112:113], v[114:115]\n" \
    "s_nop 0\n" \
    "v_add_f32 v118, v118, v119\n" \
    "s_waitcnt vmcnt(1)\n" \
    "s_nop 0\n" \
    "v_add_f32_dpp v118, v118, v118 row_ror:8 row_mask:0xf bank_mask:0xf\n" \
    "v_add_f32 v118, v118, v93\n" \
    "v_rcp_f32 v116, v118\n" \
    "s_nop 0\n" \
    "v_pk_mul_f32 v[112:113], v[112:113], v[116:117] op_sel_hi:[1,0]\n" \
    "v_pk_mul_f32 v[114:115], v[114:115], v[116:117] op_sel_hi:[1,0]\n" \
    "v_cvt_pk_bf16_f32 v120, v112, v113\n" \
    "v_cvt_pk_bf16_f32 v121, v114, v115\n" \
    "v_cvt_pk_bf16_f32 v116, v116, v116\n" \
    "v_and_b32 v124, v116, v26\n" \
    "v_mov_b32_dpp v122, v120 row_ror:8 row_mask:0xf bank_mask:0xf\n" \
    "v_mov_b32_dpp v123, v121 row_ror:8 row_mask:0xf bank_mask:0xf\n" \
    "v_and_b32 v125, v116, v27\n" \
    "v_and_b32 v126, v116, v28\n" \
    "v_mfma_f32_16x16x32_bf16 v[128:131], v[132:135], v[120:123], 0\n" \
    "v_and_b32 v127, v116, v29\n" \
    "s_waitcnt vmcnt(0)\n" \
    "s_nop 0\n" \
    "v_mfma_f32_16x16x32_bf16 v[128:131], v[88:91], v[124:127], v[128:131]\n" \
    "v_cmp_nle_f32 vcc, s80, v118\n" \
    "v_cmp_nge_f32 s[66:67], s81, v118\n" \
    "v_cmp_neq_f32 s[82:83], 0, v92\n" \
    "s_or_b64 s[66:67], vcc, s[66:67]\n" \
    "s_or_b64 s[82:83], s[82:83], s[66:67]\n" \
    "s_mov_b64 exec, s[68:69]\n" \
    "s_nop 1\n" \
    "ds_write_b128 v9, v[128:131]\n" \
    "v_cmp_u_f32 vcc, v128, v129\n" \
    "v_cmp_u_f32 s[66:67], v130, v131\n" \
    "s_mov_b64 exec, -1\n" \
    "s_or_b64 s[82:83], s[82:83], vcc\n" \
    "s_or_b64 s[82:83], s[82:83], s[66:67]\n" \
    "s_cbranch_scc1 CH_CHKV%=\n" \
    "CH_B3%=:\n" \
    "s_cmp_lg_u32 %2, 0\n" \
    "s_cbranch_scc1 CH_F1%=\n" \
    "s_waitcnt lgkmcnt(0)\n" \
    "s_barrier\n" \
    "ds_read_b64 v[112:113], v10 offset:0\n" \
    "ds_read_b64 v[114:115], v10 offset:512\n" \
    "ds_read_b64 v[116:117], v10 offset:1024\n" \
    "ds_read_b64 v[118:119], v10 offset:1536\n" \
    "s_add_u32 s52, s47, 262144\n" \
    "s_xor_b32 s52, s52, 16384\n" \
    "s_cmp_eq_u32 %10, 0\n" \
    "s_waitcnt lgkmcnt(2)\n" \
    "v_add_f32 v112, v112, v114\n" \
    "v_add_f32 v113, v113, v115\n" \
    "s_waitcnt lgkmcnt(0)\n" \
    "v_add_f32 v116, v116, v118\n" \
    "v_add_f32 v117, v117, v119\n" \
    "v_add_f32 v112, v112, v116\n" \
    "v_add_f32 v113, v113, v117\n" \
    "v_cvt_pk_bf16_f32 v112, v112, v113\n" \
    "s_cbranch_scc1 CH_F0%=\n" \
    "buffer_store_dword v112, v16, %0, s47 offen sc1\n" \
    "buffer_store_dword v112, v16, %0, s52 offen sc1\n" \
    "buffer_store_dword v112, v16, %0, s49 offen sc1\n" \
    "s_cmp_eq_u32 s40, 0\n" \
    "s_cbranch_scc1 CH_F3%=\n" \
    "buffer_store_dword v20, v16, %0, s48 offen sc1\n" \
    "s_add_u32 s52, s48, 262144\n" \
    "s_xor_b32 s52, s52, 16384\n" \
    "buffer_store_dword v20, v16, %0, s52 offen sc1\n" \
    "CH_F0%=:\n" \
    "s_cmp_eq_u32 s40, 0\n" \
    "s_cbranch_scc1 CH_F3%=\n" \
    "s_add_u32 s52, s48, 131072\n" \
    "buffer_store_dword v20, v12, %0, s52 offen sc1\n" \
    "s_branch CH_F3%=\n" \
    "CH_F1%=:\n" \
    "s_waitcnt lgkmcnt(0)\n" \
    "s_barrier\n" \
    "CH_F3%=:\n" \
    "CH_F2%=:\n" \
    "v_mov_b64 v[80:81], 0\n" \
    "v_mov_b64 v[82:83], 0\n" \
    "v_mov_b64 v[96:97], 0\n" \
    "v_mov_b64 v[98:99], 0\n" \
    "v_mov_b64 v[84:85], 0\n" \
    "v_mov_b64 v[86:87], 0\n" \
    "v_mov_b64 v[100:101], 0\n" \
    "v_mov_b64 v[102:103], 0\n" \
    "v_mov_b64 v[88:89], 0\n" \
    "v_mov_b64 v[90:91], 0\n" \
    "v_mov_b64 v[104:105], 0\n" \
    "v_mov_b64 v[106:107], 0\n" \
    "v_mov_b64 v[92:93], 0\n" \
    "v_mov_b64 v[94:95], 0\n" \
    "v_mov_b64 v[108:109], 0\n" \
    "v_mov_b64 v[110:111], 0\n" \
    "s_mov_b32 s40, s45\n" \
    "s_mov_b32 s41, s47\n" \
    "s_add_u32 s42, s47, 131072\n" \
    "s_add_u32 s44, s51, %4\n" \
    "s_add_u32 s44, s44, 524288\n" \
    "CH_NEXT%=:\n" \
    "s_cmp_lt_u32 s40, 63\n" \
    "s_cbranch_scc1 CH_POLL%=\n" \
    "s_branch CH_END%=\n" \
    "CH_LATEM%=:\n" \
    "s_sub_u32 s70, s70, 1\n" \
    "s_nop 15\n" \
    "s_nop 15\n" \
    "s_nop 15\n" \
    "s_nop 15\n" \
    "s_nop 5\n" \
    "s_branch CH_NEXT%=\n" \
    "CH_LATEV%=:\n" \
    "s_nop 15\n" \
    "s_nop 15\n" \
    "s_nop 7\n" \
    "buffer_load_dwordx4 v[80:83], v13, %0, s42 offen sc1\n" \
    "buffer_load_dwordx4 v[84:87], v13, %0, s42 offen offset:256 sc1\n" \
    "buffer_load_dwordx2 v[92:93], v14, %0, s41 offen sc1\n" \
    "buffer_load_dwordx4 v[88:91], v15, %0, s44 offen sc1\n" \
    "s_sub_u32 s70, s70, 1\n" \
    "s_branch CH_VST%=\n" \
    "CH_CHKM%=:\n" \
    "v_max3_u32 v30, v32, v33, v34\n" \
    "v_max_u32 v30, v30, v35\n" \
    "v_max3_u32 v30, v30, v36, v37\n" \
    "v_max3_u32 v30, v30, v38, v39\n" \
    "v_max3_u32 v30, v30, v40, v41\n" \
    "v_max3_u32 v30, v30, v42, v43\n" \
    "v_max3_u32 v30, v30, v44, v45\n" \
    "v_max3_u32 v30, v30, v46, v47\n" \
    "v_max3_u32 v30, v30, v48, v49\n" \
    "v_max3_u32 v30, v30, v50, v51\n" \
    "v_max3_u32 v30, v30, v52, v53\n" \
    "v_max3_u32 v30, v30, v54, v55\n" \
    "v_max3_u32 v30, v30, v56, v57\n" \
    "v_max3_u32 v30, v30, v58, v59\n" \
    "v_cmp_eq_u32 vcc, -1, v30\n" \
    "s_cbranch_vccz CH_B2%=\n" \
    "v_mov_b64 v[80:81], 0\n" \
    "v_mov_b64 v[82:83], 0\n" \
    "v_mov_b64 v[84:85], 0\n" \
    "v_mov_b64 v[86:87], 0\n" \
    "v_mov_b64 v[88:89], 0\n" \
    "v_mov_b64 v[90:91], 0\n" \
    "v_mov_b64 v[92:93], 0\n" \
    "v_mov_b64 v[94:95], 0\n" \
    "CH_RM%=:\n" \
    "s_waitcnt lgkmcnt(0)\n" \
    "s_barrier\n" \
    "s_add_u32 s46, s46, 1\n" \
    "s_add_u32 s54, s54, 1\n" \
    "s_cmp_ge_u32 s46, 262144\n" \
    "s_cbranch_scc1 CH_DEAD%=\n" \
    "s_add_u32 s70, s70, 56\n" \
    "CH_Wr%=:\n" \
    "s_memrealtime s[58:59]\n" \
    "s_waitcnt lgkmcnt(0)\n" \
    "s_sub_u32 s58, s58, s70\n" \
    "s_add_u32 s58, s58, 80\n" \
    "s_cmp_lt_i32 s58, 0\n" \
    "s_cbranch_scc1 CH_Wr%=\n" \
    "s_cmp_gt_i32 s58, 24\n" \
    "s_cbranch_scc1 CH_Xr%=\n" \
    "s_nop 9\n" \
    "s_cmp_eq_u32 %2, 0\n" \
    "s_cbranch_scc1 CH_NDb%=\n" \
    "s_add_u32 m0, %8, 0\n" \
    "s_cmp_lg_u32 %2, 1\n" \
    "buffer_load_dwordx4 v0, %0, s41 offen sc1 lds\n" \
    "buffer_load_dwordx4 v0, %0, s41 offen offset:1024 sc1 lds\n" \
    "s_cbranch_scc1 CH_NEb%=\n" \
    "s_add_u32 m0, %9, 0\n" \
    "s_nop 0\n" \
    "buffer_load_dwordx4 v1, %0, s41 offen sc1 lds\n" \
    "CH_NEb%=:\n" \
    "s_waitcnt vmcnt(0)\n" \
    "CH_NDb%=:\n" \
    "s_barrier\n" \
    "ds_read_b128 v[32:35], v2 offset:0\n" \
    "ds_read_b128 v[36:39], v2 offset:1024\n" \
    "ds_read_b128 v[40:43], v2 offset:2048\n" \
    "ds_read_b128 v[44:47], v2 offset:3072\n" \
    "ds_read_b128 v[48:51], v2 offset:4096\n" \
    "ds_read_b128 v[52:55], v2 offset:5120\n" \
    "ds_read_b128 v[56:59], v2 offset:6144\n" \
    "s_waitcnt lgkmcnt(0)\n" \
    "v_max3_u32 v30, v32, v33, v34\n" \
    "v_max_u32 v30, v30, v35\n" \
    "v_max3_u32 v30, v30, v36, v37\n" \
    "v_max3_u32 v30, v30, v38, v39\n" \
    "v_max3_u32 v30, v30, v40, v41\n" \
    "v_max3_u32 v30, v30, v42, v43\n" \
    "v_max3_u32 v30, v30, v44, v45\n" \
    "v_max3_u32 v30, v30, v46, v47\n" \
    "v_max3_u32 v30, v30, v48, v49\n" \
    "v_max3_u32 v30, v30, v50, v51\n" \
    "v_max3_u32 v30, v30, v52, v53\n" \
    "v_max3_u32 v30, v30, v54, v55\n" \
    "v_max3_u32 v30, v30, v56, v57\n" \
    "v_max3_u32 v30, v30, v58, v59\n" \
    "v_cmp_eq_u32 vcc, -1, v30\n" \
    "s_cbranch_vccnz CH_RM%=\n" \
    "s_branch CH_QK%=\n" \
    "CH_VGEN%=:\n" \
    "s_nop 3\n" \
    "v_pk_add_f32 v[112:113], v[80:81], v[84:85]\n" \
    "v_pk_add_f32 v[114:115], v[82:83], v[86:87]\n" \
    "v_max3_f32 v116, v112, v113, v92\n" \
    "v_max3_f32 v116, v116, v114, v115\n" \
    "s_nop 1\n" \
    "v_max_f32_dpp v116, v116, v116 row_ror:8 row_mask:0xf bank_mask:0xf\n" \
    "v_pk_add_f32 v[112:113], v[112:113], v[116:117] op_sel_hi:[1,0] neg_lo:[0,1] neg_hi:[0,1]\n" \
    "v_pk_add_f32 v[114:115], v[114:115], v[116:117] op_sel_hi:[1,0] neg_lo:[0,1] neg_hi:[0,1]\n" \
    "v_sub_f32 v117, v92, v116\n" \
    "v_exp_f32 v112, v112\n" \
    "v_exp_f32 v113, v113\n" \
    "v_exp_f32 v114, v114\n" \
    "v_exp_f32 v115, v115\n" \
    "v_exp_f32 v117, v117\n" \
    "v_pk_add_f32 v[118:119], v[112:113], v[114:115]\n" \
    "s_nop 0\n" \
    "v_add_f32 v118, v118, v119\n" \
    "s_nop 1\n" \
    "v_add_f32_dpp v118, v118, v118 row_ror:8 row_mask:0xf bank_mask:0xf\n" \
    "v_fmac_f32 v118, v93, v117\n" \
    "v_rcp_f32 v118, v118\n" \
    "s_nop 0\n" \
    "v_pk_mul_f32 v[112:113], v[112:113], v[118:119] op_sel_hi:[1,0]\n" \
    "v_pk_mul_f32 v[114:115], v[114:115], v[118:119] op_sel_hi:[1,0]\n" \
    "v_mul_f32 v117, v117, v118\n" \
    "v_cvt_pk_bf16_f32 v120, v112, v113\n" \
    "v_cvt_pk_bf16_f32 v121, v114, v115\n" \
    "v_cvt_pk_bf16_f32 v117, v117, v117\n" \
    "v_and_b32 v124, v117, v26\n" \
    "v_mov_b32_dpp v122, v120 row_ror:8 row_mask:0xf bank_mask:0xf\n" \
    "v_mov_b32_dpp v123, v121 row_ror:8 row_mask:0xf bank_mask:0xf\n" \
    "v_and_b32 v125, v117, v27\n" \
    "v_and_b32 v126, v117, v28\n" \
    "v_mfma_f32_16x16x32_bf16 v[128:131], v[132:135], v[120:123], 0\n" \
    "v_and_b32 v127, v117, v29\n" \
    "s_waitcnt vmcnt(0)\n" \
    "s_nop 0\n" \
    "v_mfma_f32_16x16x32_bf16 v[128:131], v[88:91], v[124:127], v[128:131]\n" \
    "s_nop 4\n" \
    "s_mov_b64 exec, s[68:69]\n" \
    "s_nop 1\n" \
    "ds_write_b128 v9, v[128:131]\n" \
    "s_mov_b64 exec, -1\n" \
    "s_branch CH_B3%=\n" \
    "CH_CHKV%=:\n" \
    "v_max3_u32 v30, v80, v81, v82\n" \
    "v_max3_u32 v30, v30, v83, v84\n" \
    "v_max3_u32 v30, v30, v85, v86\n" \
    "v_max_u32 v30, v30, v87\n" \
    "v_max3_u32 v30, v30, v92, v93\n" \
    "v_max3_u32 v30, v30, v88, v89\n" \
    "v_max3_u32 v30, v30, v90, v91\n" \
    "v_cmp_eq_u32 vcc, -1, v30\n" \
    "s_cbranch_vccz CH_VGEN%=\n" \
    "CH_RV%=:\n" \
    "s_add_u32 s70, s70, 56\n" \
    "CH_Wv%=:\n" \
    "s_memrealtime s[58:59]\n" \
    "s_waitcnt lgkmcnt(0)\n" \
    "s_sub_u32 s58, s58, s70\n" \
    "s_add_u32 s58, s58, 68\n" \
    "s_cmp_lt_i32 s58, 0\n" \
    "s_cbranch_scc1 CH_Wv%=\n" \
    "s_cmp_gt_i32 s58, 24\n" \
    "s_cbranch_scc1 CH_Xv%=\n" \
    "buffer_load_dwordx4 v[80:83], v13, %0, s42 offen sc1\n" \
    "buffer_load_dwordx4 v[84:87], v13, %0, s42 offen offset:256 sc1\n" \
    "buffer_load_dwordx2 v[92:93], v14, %0, s41 offen sc1\n" \
    "buffer_load_dwordx4 v[88:91], v15, %0, s44 offen sc1\n" \
    "s_add_u32 s46, s46, 1\n" \
    "s_add_u32 s55, s55, 1\n" \
    "s_cmp_ge_u32 s46, 262144\n" \
    "s_cbranch_scc1 CH_DEAD%=\n" \
    "s_waitcnt vmcnt(0)\n" \
    "v_max3_u32 v30, v80, v81, v82\n" \
    "v_max3_u32 v30, v30, v83, v84\n" \
    "v_max3_u32 v30, v30, v85, v86\n" \
    "v_max_u32 v30, v30, v87\n" \
    "v_max3_u32 v30, v30, v92, v93\n" \
    "v_max3_u32 v30, v30, v88, v89\n" \
    "v_max3_u32 v30, v30, v90, v91\n" \
    "v_cmp_eq_u32 vcc, -1, v30\n" \
    "s_cbranch_vccnz CH_RV%=\n" \
    "s_branch CH_VST%=\n" \
    "CH_Xm%=:\n" \
    "s_add_u32 s70, s70, 56\n" \
    "s_add_u32 s87, s87, 1\n" \
    "s_branch CH_Wm%=\n" \
    "CH_Xsa%=:\n" \
    "s_add_u32 s70, s70, 56\n" \
    "s_add_u32 s87, s87, 1\n" \
    "s_branch CH_Wsa%=\n" \
    "CH_Xsb%=:\n" \
    "s_add_u32 s70, s70, 56\n" \
    "s_add_u32 s87, s87, 1\n" \
    "s_branch CH_Wsb%=\n" \
    "CH_Xr%=:\n" \
    "s_add_u32 s70, s70, 56\n" \
    "s_add_u32 s87, s87, 1\n" \
    "s_branch CH_Wr%=\n" \
    "CH_Xv%=:\n" \
    "s_add_u32 s70, s70, 56\n" \
    "s_add_u32 s87, s87, 1\n" \
    "s_branch CH_Wv%=\n" \
    "CH_DEAD%=:\n" \
    "v_mov_b32 v112, 2228480\n" \
    "v_mov_b32 v113, 2\n" \
    "buffer_store_dword v113, v112, %0, 0 offen sc1\n" \
    "CH_END%=:\n" \
    "MEGA_CHAIN_END:\n" \
    "s_waitcnt vmcnt(0)\n" \
    "s_setprio 0\n"

#define CHAIN_CLOB "v0", "v1", "v2", "v3", "v4", "v5", "v6", "v7", "v8", "v9", "v10", "v11", "v12", "v13", "v14", "v15", "v16", "v17", "v18", "v19", "v20", "v21", "v22", "v23", "v24", "v25", "v26", "v27", "v28", "v29", "v30", "v31", "v32", "v33", "v34", "v35", "v36", "v37", "v38", "v39", "v40", "v41", "v42", "v43", "v44", "v45", "v46", "v47", "v48", "v49", "v50", "v51", "v52", "v53", "v54", "v55", "v56", "v57", "v58", "v59", "v60", "v61", "v62", "v63", "v64", "v65", "v66", "v67", "v68", "v69", "v70", "v71", "v72", "v73", "v74", "v75", "v76", "v77", "v78", "v79", "v80", "v81", "v82", "v83", "v84", "v85", "v86", "v87", "v88", "v89", "v90", "v91", "v92", "v93", "v94", "v95", "v96", "v97", "v98", "v99", "v100", "v101", "v102", "v103", "v104", "v105", "v106", "v107", "v108", "v109", "v110", "v111", "v112", "v113", "v114", "v115", "v116", "v117", "v118", "v119", "v120", "v121", "v122", "v123", "v124", "v125", "v126", "v127", "v128", "v129", "v130", "v131", "v132", "v133", "v134", "v135", "v136", "v137", "v138", "v139", "v140", "v141", "v142", "v143", "v144", "v145", "v146", "v147", "v148", "v149", "v150", "v151", "v152", "v153", "v154", "v155", "v156", "v157", "v158", "v159", "v160", "v161", "v162", "v163", "v164", "v165", "v166", "v167", "v168", "v169", "v170", "v171", "v172", "v173", "v174", "v175", "v176", "v177", "v178", "v179", "v180", "v181", "v182", "v183", "v184", "v185", "v186", "v187", "v188", "v189", "v190", "v191", "v192", "v193", "v194", "v195", "v196", "v197", "v198", "v199", "v200", "v201", "v202", "v203", "v204", "v205", "v206", "v207", "v208", "v209", "v210", "v211", "v212", "v213", "v214", "v215", "v216", "v217", "v218", "v219", "v220", "v221", "v222", "v223", "v224", "v225", "v226", "v227", "v228", "v229", "v230", "v231", "v232", "v233", "v234", "v235", "v236", "v237", "v238", "v239", "v240", "v241", "v242", "v243", "v244", "v245", "v246", "v247", "v248", "v249", "v250", "v251", "v252", "v253", "v254", "v255", "a0", "a1", "a2", "a3", "a4", "a5", "a6", "a7", "a8", "a9", "a10", "a11", "a12", "a13", "a14", "a15", "a16", "a17", "a18", "a19", "a20", "a21", "a22", "a23", "a24", "a25", "a26", "a27", "a28", "a29", "a30", "a31", "a32", "a33", "a34", "a35", "a36", "a37", "a38", "a39", "a40", "a41", "a42", "a43", "a44", "a45", "a46", "a47", "a48", "a49", "a50", "a51", "a52", "a53", "a54", "a55", "a56", "a57", "a58", "a59", "a60", "a61", "a62", "a63", "a64", "a65", "a66", "a67", "a68", "a69", "a70", "a71", "a72", "a73", "a74", "a75", "a76", "a77", "a78", "a79", "a80", "a81", "a82", "a83", "a84", "a85", "a86", "a87", "a88", "a89", "a90", "a91", "a92", "a93", "a94", "a95", "a96", "a97", "a98", "a99", "a100", "a101", "a102", "a103", "a104", "a105", "a106", "a107", "a108", "a109", "a110", "a111", "a112", "a113", "a114", "a115", "a116", "a117", "a118", "a119", "a120", "a121", "a122", "a123", "a124", "a125", "a126", "a127", "a128", "a129", "a130", "a131", "a132", "a133", "a134", "a135", "a136", "a137", "a138", "a139", "a140", "a141", "a142", "a143", "a144", "a145", "a146", "a147", "a148", "a149", "a150", "a151", "a152", "a153", "a154", "a155", "a156", "a157", "a158", "a159", "a160", "a161", "a162", "a163", "a164", "a165", "a166", "a167", "a168", "a169", "a170", "a171", "a172", "a173", "a174", "a175", "a176", "a177", "a178", "a179", "a180", "a181", "a182", "a183", "a184", "a185", "a186", "a187", "a188", "a189", "a190", "a191", "a192", "a193", "a194", "a195", "a196", "a197", "a198", "a199", "a200", "a201", "a202", "a203", "a204", "a205", "a206", "a207", "a208", "a209", "a210", "a211", "a212", "a213", "a214", "a215", "a216", "a217", "a218", "a219", "a220", "a221", "a222", "a223", "a224", "a225", "a226", "a227", "a228", "a229", "a230", "a231", "a232", "a233", "a234", "a235", "a236", "a237", "a238", "a239", "a240", "a241", "a242", "a243", "a244", "a245", "a246", "a247", "a248", "a249", "a250", "a251", "a252", "a253", "a254", "a255", "s40", "s41", "s42", "s43", "s44", "s45", "s46", "s47", "s48", "s49", "s50", "s51", "s52", "s53", "s54", "s55", "s56", "s57", "s58", "s59", "s60", "s61", "s62", "s63", "s64", "s65", "s66", "s67", "s68", "s69", "s70", "s71", "s72", "s73", "s74", "s75", "s76", "s77", "s78", "s79", "s80", "s81", "s82", "s83", "s84", "s85", "s86", "s87", "s88", "s89", "s90", "s91", "s92", "s93", "s94", "s95", "vcc", "scc", "memory"

#define X_ASM \
    "v_mbcnt_lo_u32_b32 v90, -1, 0\n" \
    "v_mbcnt_hi_u32_b32 v90, -1, v90\n" \
    "v_lshlrev_b32 v90, 2, v90\n" \
    "v_add_u32 v90, %1, v90\n" \
    "ds_read_b32 v0, v90 offset:0\n" \
    "ds_read_b32 v1, v90 offset:1024\n" \
    "ds_read_b32 v2, v90 offset:2048\n" \
    "ds_read_b32 v3, v90 offset:3072\n" \
    "ds_read_b32 v4, v90 offset:4096\n" \
    "ds_read_b32 v5, v90 offset:5120\n" \
    "ds_read_b32 v6, v90 offset:6144\n" \
    "ds_read_b32 v7, v90 offset:7168\n" \
    "ds_read_b32 v8, v90 offset:8192\n" \
    "ds_read_b32 v9, v90 offset:9216\n" \
    "ds_read_b32 v10, v90 offset:10240\n" \
    "ds_read_b32 v11, v90 offset:11264\n" \
    "ds_read_b32 v12, v90 offset:12288\n" \
    "ds_read_b32 v13, v90 offset:13312\n" \
    "ds_read_b32 v16, v90 offset:14336\n" \
    "ds_read_b32 v17, v90 offset:15360\n" \
    "ds_read_b32 v18, v90 offset:16384\n" \
    "ds_read_b32 v19, v90 offset:17408\n" \
    "v_mov_b32 v20, -1\n" \
    "v_mov_b32 v21, -1\n" \
    "v_mov_b32 v22, -1\n" \
    "v_mov_b32 v23, -1\n" \
    "s_waitcnt lgkmcnt(0)\n" \
    "s_mov_b32 s40, 0\n" \
    "s_mov_b32 s46, 0\n" \
    "s_mov_b32 s54, 0\n" \
    "s_mov_b32 s42, %5\n" \
    "s_mov_b32 s43, %6\n" \
    "s_mov_b32 s86, 0\n" \
    "v_not_b32 v14, %9\n" \
    "X_FLa%=:\n" \
    "v_cmp_ne_u32 vcc, %9, v14\n" \
    "v_cmp_ne_u32 s[66:67], %10, v15\n" \
    "s_or_b64 vcc, vcc, s[66:67]\n" \
    "s_cbranch_scc0 X_FKa%=\n" \
    "s_sleep 2\n" \
    "s_bitset1_b64 s[92:93], s40\n" \
    "buffer_load_dwordx2 v[14:15], v12, %0, s86 offen sc1\n" \
    "s_add_u32 s46, s46, 1\n" \
    "s_cmp_ge_u32 s46, 262144\n" \
    "s_cbranch_scc1 X_DEAD%=\n" \
    "s_waitcnt vmcnt(0)\n" \
    "s_branch X_FLa%=\n" \
    "X_FKa%=:\n" \
    "s_add_u32 s86, s86, 2048\n" \
    "s_add_u32 s45, s42, 14336\n" \
    "buffer_load_dwordx4 v[26:29], v2, %0, s42 offen sc1\n" \
    "buffer_load_dwordx4 v[30:33], v2, %0, s42 offen offset:1024 sc1\n" \
    "buffer_load_dwordx4 v[34:37], v3, %0, s42 offen sc1\n" \
    "buffer_load_dwordx4 v[38:41], v3, %0, s42 offen offset:1024 sc1\n" \
    "buffer_load_dwordx4 v[42:45], v2, %0, s45 offen sc1\n" \
    "buffer_load_dwordx4 v[46:49], v2, %0, s45 offen offset:1024 sc1\n" \
    "buffer_load_dwordx4 v[50:53], v3, %0, s45 offen sc1\n" \
    "buffer_load_dwordx4 v[54:57], v3, %0, s45 offen offset:1024 sc1\n" \
    "buffer_load_dwordx4 v[58:61], v4, %0, s43 offen offset:0 sc1\n" \
    "buffer_load_dwordx4 v[62:65], v4, %0, s43 offen offset:1024 sc1\n" \
    "buffer_load_dwordx4 v[66:69], v4, %0, s43 offen offset:2048 sc1\n" \
    "buffer_load_dwordx4 v[70:73], v4, %0, s43 offen offset:3072 sc1\n" \
    "buffer_load_dwordx2 v[14:15], v12, %0, s86 offen sc1\n" \
    "s_add_u32 s41, %3, 262144\n" \
    "s_xor_b32 s41, s41, 16384\n" \
    "s_mov_b32 s47, %3\n" \
    "s_add_u32 s44, %4, 524288\n" \
    "s_mov_b32 s55, 0\n" \
    "s_mov_b32 s80, 0x21800000\n" \
    "s_mov_b32 s81, 0x5d800000\n" \
    "s_mov_b32 s84, -1\n" \
    "s_mov_b32 s85, 0\n" \
    "s_setprio 3\n" \
    "s_mov_b64 s[90:91], 0\n" \
    "s_mov_b64 s[92:93], 0\n" \
    "s_mov_b32 s53, 0\n" \
    "s_memtime s[60:61]\n" \
    "s_waitcnt lgkmcnt(0)\n" \
    "X_TOP%=:\n" \
    "s_waitcnt vmcnt(0) lgkmcnt(0)\n" \
    "s_xor_b32 s49, %3, 16384\n" \
    "buffer_load_dwordx2 v[92:93], v13, %0, s49 offen sc1\n" \
    "v_and_b32 v34, v34, v10\n" \
    "v_and_b32 v35, v35, v10\n" \
    "v_and_b32 v36, v36, v10\n" \
    "v_and_b32 v37, v37, v10\n" \
    "v_and_b32 v38, v38, v10\n" \
    "v_and_b32 v39, v39, v10\n" \
    "v_and_b32 v40, v40, v10\n" \
    "v_and_b32 v41, v41, v10\n" \
    "v_and_b32 v50, v50, v10\n" \
    "v_and_b32 v51, v51, v10\n" \
    "v_and_b32 v52, v52, v10\n" \
    "v_and_b32 v53, v53, v10\n" \
    "v_and_b32 v54, v54, v10\n" \
    "v_and_b32 v55, v55, v10\n" \
    "v_and_b32 v56, v56, v10\n" \
    "v_and_b32 v57, v57, v10\n" \
    "X_FLb%=:\n" \
    "v_cmp_ne_u32 vcc, %9, v14\n" \
    "v_cmp_ne_u32 s[66:67], %10, v15\n" \
    "s_or_b64 vcc, vcc, s[66:67]\n" \
    "s_cbranch_scc0 X_FKb%=\n" \
    "s_sleep 2\n" \
    "s_bitset1_b64 s[92:93], s40\n" \
    "buffer_load_dwordx2 v[14:15], v12, %0, s86 offen sc1\n" \
    "s_add_u32 s46, s46, 1\n" \
    "s_cmp_ge_u32 s46, 262144\n" \
    "s_cbranch_scc1 X_DEAD%=\n" \
    "s_waitcnt vmcnt(0)\n" \
    "s_branch X_FLb%=\n" \
    "X_FKb%=:\n" \
    "s_add_u32 s86, s86, 2048\n" \
    "s_add_u32 s42, s42, 458752\n" \
    "s_add_u32 s45, s42, 14336\n" \
    "s_add_u32 s43, s43, 458752\n" \
    "v_mov_b64 v[82:83], 0\n" \
    "v_mov_b64 v[84:85], 0\n" \
    "v_mov_b64 v[86:87], 0\n" \
    "v_mov_b64 v[88:89], 0\n" \
    "s_waitcnt vmcnt(0)\n" \
    "v_readfirstlane_b32 s48, v92\n" \
    "v_readfirstlane_b32 s49, v93\n" \
    "s_cmp_lg_u32 s49, s40\n" \
    "s_cbranch_scc1 X_BD%=\n" \
    "X_BW%=:\n" \
    "s_memrealtime s[58:59]\n" \
    "s_waitcnt lgkmcnt(0)\n" \
    "s_sub_u32 s58, s58, s48\n" \
    "s_add_u32 s58, s58, 256\n" \
    "s_cmp_lt_u32 s58, 260\n" \
    "s_cbranch_scc1 X_BW%=\n" \
    "X_BD%=:\n" \
    "buffer_load_dwordx4 v[74:77], v0, %0, s41 offen sc1\n" \
    "buffer_load_dwordx4 v[78:81], v1, %0, s41 offen sc1\n" \
    "s_nop 15\n" \
    "s_nop 7\n" \
    "buffer_load_dwordx4 v[130:133], v0, %0, s41 offen sc1\n" \
    "buffer_load_dwordx4 v[134:137], v1, %0, s41 offen sc1\n" \
    "s_waitcnt vmcnt(2)\n" \
    "v_smfmac_f32_16x16x64_bf16 v[82:85], v[74:77], v[26:33], v9\n" \
    "v_max3_u32 v24, v74, v75, v76\n" \
    "v_smfmac_f32_16x16x64_bf16 v[86:89], v[74:77], v[42:49], v9\n" \
    "v_max3_u32 v24, v24, v77, v78\n" \
    "v_smfmac_f32_16x16x64_bf16 v[82:85], v[78:81], v[34:41], v9\n" \
    "v_max3_u32 v24, v24, v79, v80\n" \
    "v_smfmac_f32_16x16x64_bf16 v[86:89], v[78:81], v[50:57], v9\n" \
    "v_max_u32 v24, v24, v81\n" \
    "v_cmp_eq_u32 vcc, -1, v24\n" \
    "s_cbranch_vccnz X_MISS%=\n" \
    "X_OK1%=:\n" \
    "buffer_load_dwordx4 v[26:29], v2, %0, s42 offen sc1\n" \
    "buffer_load_dwordx4 v[30:33], v2, %0, s42 offen offset:1024 sc1\n" \
    "s_nop 2\n" \
    "v_cvt_pk_bf16_f32 v90, v82, v83\n" \
    "v_cvt_pk_bf16_f32 v91, v84, v85\n" \
    "v_cvt_pk_bf16_f32 v92, v86, v87\n" \
    "v_cvt_pk_bf16_f32 v93, v88, v89\n" \
    "s_nop 1\n" \
    "v_mfma_f32_16x16x32_bf16 v[94:97], v[90:93], v[16:19], 0\n" \
    "s_nop 7\n" \
    "ds_write_b128 v5, v[94:97]\n" \
    "buffer_load_dwordx4 v[34:37], v3, %0, s42 offen sc1\n" \
    "buffer_load_dwordx4 v[38:41], v3, %0, s42 offen offset:1024 sc1\n" \
    "s_waitcnt lgkmcnt(0)\n" \
    "s_barrier\n" \
    "ds_read_b128 v[98:101], v6 offset:0\n" \
    "ds_read_b128 v[102:105], v6 offset:1024\n" \
    "ds_read_b128 v[106:109], v6 offset:2048\n" \
    "ds_read_b128 v[110:113], v6 offset:3072\n" \
    "buffer_load_dwordx4 v[42:45], v2, %0, s45 offen sc1\n" \
    "buffer_load_dwordx4 v[46:49], v2, %0, s45 offen offset:1024 sc1\n" \
    "v_mov_b32 v90, 0\n" \
    "s_waitcnt lgkmcnt(2)\n" \
    "v_pk_add_f32 v[98:99], v[98:99], v[102:103]\n" \
    "v_pk_add_f32 v[100:101], v[100:101], v[104:105]\n" \
    "s_waitcnt lgkmcnt(0)\n" \
    "v_pk_add_f32 v[106:107], v[106:107], v[110:111]\n" \
    "v_pk_add_f32 v[108:109], v[108:109], v[112:113]\n" \
    "v_pk_add_f32 v[98:99], v[98:99], v[106:107]\n" \
    "v_pk_add_f32 v[100:101], v[100:101], v[108:109]\n" \
    "v_exp_f32 v98, v98\n" \
    "v_exp_f32 v99, v99\n" \
    "v_exp_f32 v100, v100\n" \
    "v_exp_f32 v101, v101\n" \
    "v_cvt_pk_bf16_f32 v94, v98, v99\n" \
    "v_cvt_pk_bf16_f32 v95, v100, v101\n" \
    "v_add_f32 v91, v98, v99\n" \
    "v_add_f32 v92, v100, v101\n" \
    "v_mov_b32_dpp v96, v94 row_ror:8 row_mask:0xf bank_mask:0xf\n" \
    "v_mov_b32_dpp v97, v95 row_ror:8 row_mask:0xf bank_mask:0xf\n" \
    "v_add_f32 v91, v91, v92\n" \
    "s_nop 0\n" \
    "v_mfma_f32_16x16x32_bf16 v[114:117], v[94:97], v[58:61], 0\n" \
    "buffer_load_dwordx4 v[50:53], v3, %0, s45 offen sc1\n" \
    "buffer_load_dwordx4 v[54:57], v3, %0, s45 offen offset:1024 sc1\n" \
    "v_add_f32_dpp v91, v91, v91 row_ror:8 row_mask:0xf bank_mask:0xf\n" \
    "v_mov_b32 v92, v91\n" \
    "v_mfma_f32_16x16x32_bf16 v[118:121], v[94:97], v[62:65], 0\n" \
    "s_nop 0\n" \
    "v_permlane16_swap_b32 v91, v92\n" \
    "v_add_f32 v91, v91, v92\n" \
    "v_mfma_f32_16x16x32_bf16 v[122:125], v[94:97], v[66:69], 0\n" \
    "v_mov_b32 v92, v91\n" \
    "v_mfma_f32_16x16x32_bf16 v[126:129], v[94:97], v[70:73], 0\n" \
    "s_nop 0\n" \
    "v_permlane32_swap_b32 v91, v92\n" \
    "v_add_f32 v91, v91, v92\n" \
    "v_cmp_nle_f32 vcc, s80, v91\n" \
    "v_cmp_nge_f32 s[66:67], s81, v91\n" \
    "s_or_b64 vcc, vcc, s[66:67]\n" \
    "s_cbranch_scc1 X_GEN%=\n" \
    "X_G2%=:\n" \
    "s_mov_b64 exec, %7\n" \
    "buffer_store_dwordx2 v[90:91], v7, %0, s47 offen sc1\n" \
    "s_mov_b64 exec, -1\n" \
    "v_cvt_pk_bf16_f32 v98, v114, v115\n" \
    "v_cvt_pk_bf16_f32 v99, v116, v117\n" \
    "v_cvt_pk_bf16_f32 v100, v118, v119\n" \
    "v_cvt_pk_bf16_f32 v101, v120, v121\n" \
    "v_cvt_pk_bf16_f32 v102, v122, v123\n" \
    "v_cvt_pk_bf16_f32 v103, v124, v125\n" \
    "v_permlane16_swap_b32 v98, v100\n" \
    "v_permlane16_swap_b32 v99, v101\n" \
    "v_cvt_pk_bf16_f32 v104, v126, v127\n" \
    "v_cvt_pk_bf16_f32 v105, v128, v129\n" \
    "s_mov_b64 exec, s[84:85]\n" \
    "buffer_store_dwordx4 v[98:101], v8, %0, s44 offen sc1\n" \
    "s_mov_b64 exec, -1\n" \
    "v_permlane16_swap_b32 v102, v104\n" \
    "v_permlane16_swap_b32 v103, v105\n" \
    "s_mov_b64 exec, s[84:85]\n" \
    "buffer_store_dwordx4 v[102:105], v11, %0, s44 offen sc1\n" \
    "s_mov_b64 exec, -1\n" \
    "v_xor_b32 v5, 4096, v5\n" \
    "v_xor_b32 v6, 4096, v6\n" \
    "s_cmp_eq_u32 %2, 0\n" \
    "s_cbranch_scc1 X_NK%=\n" \
    "s_lshl_b32 s48, s40, 6\n" \
    "v_mov_b32 v106, 2260992\n" \
    "v_mov_b32 v108, %9\n" \
    "v_mov_b32 v109, %10\n" \
    "s_mov_b64 exec, 1\n" \
    "buffer_store_dwordx2 v[108:109], v106, %0, s48 offen sc1\n" \
    "s_mov_b64 exec, -1\n" \
    "X_NK%=:\n" \
    "s_add_u32 s48, s40, 2\n" \
    "s_and_b32 s48, s48, 3\n" \
    "s_mul_i32 s49, s48, 229376\n" \
    "s_add_u32 s49, s49, %4\n" \
    "s_add_u32 s49, s49, 524288\n" \
    "s_lshl_b32 s48, s48, 15\n" \
    "s_add_u32 s48, s48, %3\n" \
    "s_cmp_lt_u32 s40, 2\n" \
    "s_cbranch_scc1 X_NR%=\n" \
    "buffer_store_dwordx4 v[20:23], v8, %0, s49 offen sc1\n" \
    "s_mov_b64 exec, %7\n" \
    "buffer_store_dwordx2 v[20:21], v7, %0, s48 offen sc1\n" \
    "s_mov_b64 exec, -1\n" \
    "X_NR%=:\n" \
    "s_add_u32 s40, s40, 1\n" \
    "s_cmp_eq_u32 s40, 63\n" \
    "s_cbranch_scc1 X_END%=\n" \
    "buffer_load_dwordx4 v[58:61], v4, %0, s43 offen offset:0 sc1\n" \
    "buffer_load_dwordx4 v[62:65], v4, %0, s43 offen offset:1024 sc1\n" \
    "buffer_load_dwordx4 v[66:69], v4, %0, s43 offen offset:2048 sc1\n" \
    "buffer_load_dwordx4 v[70:73], v4, %0, s43 offen offset:3072 sc1\n" \
    "buffer_load_dwordx2 v[14:15], v12, %0, s86 offen sc1\n" \
    "s_and_b32 s48, s40, 3\n" \
    "s_mul_i32 s44, s48, 229376\n" \
    "s_add_u32 s44, s44, %4\n" \
    "s_add_u32 s44, s44, 524288\n" \
    "s_lshl_b32 s48, s48, 15\n" \
    "s_add_u32 s47, s48, %3\n" \
    "s_add_u32 s41, s47, 262144\n" \
    "s_xor_b32 s41, s41, 16384\n" \
    "s_branch X_TOP%=\n" \
    "X_GEN%=:\n" \
    "s_nop 7\n" \
    "ds_read_b128 v[98:101], v6 offset:0\n" \
    "ds_read_b128 v[102:105], v6 offset:1024\n" \
    "ds_read_b128 v[106:109], v6 offset:2048\n" \
    "ds_read_b128 v[110:113], v6 offset:3072\n" \
    "v_mov_b32 v90, 0\n" \
    "s_waitcnt lgkmcnt(2)\n" \
    "v_pk_add_f32 v[98:99], v[98:99], v[102:103]\n" \
    "v_pk_add_f32 v[100:101], v[100:101], v[104:105]\n" \
    "s_waitcnt lgkmcnt(0)\n" \
    "v_pk_add_f32 v[106:107], v[106:107], v[110:111]\n" \
    "v_pk_add_f32 v[108:109], v[108:109], v[112:113]\n" \
    "v_pk_add_f32 v[98:99], v[98:99], v[106:107]\n" \
    "v_pk_add_f32 v[100:101], v[100:101], v[108:109]\n" \
    "s_nop 0\n" \
    "v_max3_f32 v90, v98, v99, v100\n" \
    "v_max_f32 v90, v90, v101\n" \
    "s_nop 1\n" \
    "v_max_f32_dpp v90, v90, v90 row_ror:8 row_mask:0xf bank_mask:0xf\n" \
    "v_mov_b32 v92, v90\n" \
    "s_nop 1\n" \
    "v_permlane16_swap_b32 v90, v92\n" \
    "v_max_f32 v90, v90, v92\n" \
    "v_mov_b32 v92, v90\n" \
    "s_nop 1\n" \
    "v_permlane32_swap_b32 v90, v92\n" \
    "v_max_f32 v90, v90, v92\n" \
    "v_pk_add_f32 v[98:99], v[98:99], v[90:91] op_sel_hi:[1,0] neg_lo:[0,1] neg_hi:[0,1]\n" \
    "v_pk_add_f32 v[100:101], v[100:101], v[90:91] op_sel_hi:[1,0] neg_lo:[0,1] neg_hi:[0,1]\n" \
    "v_exp_f32 v98, v98\n" \
    "v_exp_f32 v99, v99\n" \
    "v_exp_f32 v100, v100\n" \
    "v_exp_f32 v101, v101\n" \
    "v_cvt_pk_bf16_f32 v94, v98, v99\n" \
    "v_cvt_pk_bf16_f32 v95, v100, v101\n" \
    "v_add_f32 v91, v98, v99\n" \
    "v_add_f32 v92, v100, v101\n" \
    "v_mov_b32_dpp v96, v94 row_ror:8 row_mask:0xf bank_mask:0xf\n" \
    "v_mov_b32_dpp v97, v95 row_ror:8 row_mask:0xf bank_mask:0xf\n" \
    "v_add_f32 v91, v91, v92\n" \
    "s_nop 0\n" \
    "v_mfma_f32_16x16x32_bf16 v[114:117], v[94:97], v[58:61], 0\n" \
    "v_add_f32_dpp v91, v91, v91 row_ror:8 row_mask:0xf bank_mask:0xf\n" \
    "v_mov_b32 v92, v91\n" \
    "v_mfma_f32_16x16x32_bf16 v[118:121], v[94:97], v[62:65], 0\n" \
    "s_nop 0\n" \
    "v_permlane16_swap_b32 v91, v92\n" \
    "v_add_f32 v91, v91, v92\n" \
    "v_mfma_f32_16x16x32_bf16 v[122:125], v[94:97], v[66:69], 0\n" \
    "v_mov_b32 v92, v91\n" \
    "v_mfma_f32_16x16x32_bf16 v[126:129], v[94:97], v[70:73], 0\n" \
    "s_nop 0\n" \
    "v_permlane32_swap_b32 v91, v92\n" \
    "v_add_f32 v91, v91, v92\n" \
    "s_nop 7\n" \
    "s_branch X_G2%=\n" \
    "X_MISS%=:\n" \
    "s_nop 7\n" \
    "v_mov_b64 v[82:83], 0\n" \
    "v_mov_b64 v[84:85], 0\n" \
    "v_mov_b64 v[86:87], 0\n" \
    "v_mov_b64 v[88:89], 0\n" \
    "s_branch X_L1%=\n" \
    "X_LP%=:\n" \
    "buffer_load_dwordx4 v[130:133], v0, %0, s41 offen sc1\n" \
    "buffer_load_dwordx4 v[134:137], v1, %0, s41 offen sc1\n" \
    "s_waitcnt vmcnt(2)\n" \
    "v_max3_u32 v24, v74, v75, v76\n" \
    "v_max3_u32 v90, v78, v79, v80\n" \
    "v_max3_u32 v24, v24, v77, v81\n" \
    "v_max_u32 v24, v24, v90\n" \
    "v_cmp_eq_u32 vcc, -1, v24\n" \
    "s_cbranch_vccz X_U0%=\n" \
    "s_nop 3\n" \
    "X_L1%=:\n" \
    "buffer_load_dwordx4 v[74:77], v0, %0, s41 offen sc1\n" \
    "buffer_load_dwordx4 v[78:81], v1, %0, s41 offen sc1\n" \
    "s_waitcnt vmcnt(2)\n" \
    "v_max3_u32 v24, v130, v131, v132\n" \
    "v_max3_u32 v90, v134, v135, v136\n" \
    "v_max3_u32 v24, v24, v133, v137\n" \
    "v_max_u32 v24, v24, v90\n" \
    "v_cmp_eq_u32 vcc, -1, v24\n" \
    "s_cbranch_vccz X_U1%=\n" \
    "s_nop 3\n" \
    "s_add_u32 s46, s46, 1\n" \
    "s_add_u32 s55, s55, 1\n" \
    "s_cmp_lt_u32 s46, 262144\n" \
    "s_cbranch_scc1 X_LP%=\n" \
    "s_branch X_DEAD%=\n" \
    "X_U1%=:\n" \
    "v_smfmac_f32_16x16x64_bf16 v[82:85], v[130:133], v[26:33], v9\n" \
    "v_smfmac_f32_16x16x64_bf16 v[86:89], v[130:133], v[42:49], v9\n" \
    "v_smfmac_f32_16x16x64_bf16 v[82:85], v[134:137], v[34:41], v9\n" \
    "v_smfmac_f32_16x16x64_bf16 v[86:89], v[134:137], v[50:57], v9\n" \
    "s_nop 0\n" \
    "s_branch X_OK%=\n" \
    "X_U0%=:\n" \
    "v_smfmac_f32_16x16x64_bf16 v[82:85], v[74:77], v[26:33], v9\n" \
    "v_smfmac_f32_16x16x64_bf16 v[86:89], v[74:77], v[42:49], v9\n" \
    "v_smfmac_f32_16x16x64_bf16 v[82:85], v[78:81], v[34:41], v9\n" \
    "v_smfmac_f32_16x16x64_bf16 v[86:89], v[78:81], v[50:57], v9\n" \
    "s_nop 0\n" \
    "X_OK%=:\n" \
    "s_nop 1\n" \
    "s_branch X_OK1%=\n" \
    "X_DEAD%=:\n" \
    "v_mov_b32 v90, 2228480\n" \
    "v_mov_b32 v91, 3\n" \
    "buffer_store_dword v91, v90, %0, 0 offen sc1\n" \
    "X_END%=:\n" \
    "s_waitcnt vmcnt(0)\n" \
    "s_setprio 0\n"

#define X_CLOB "v0", "v1", "v2", "v3", "v4", "v5", "v6", "v7", "v8", "v9", "v10", "v11", "v12", "v13", "v14", "v15", "v16", "v17", "v18", "v19", "v20", "v21", "v22", "v23", "v24", "v25", "v26", "v27", "v28", "v29", "v30", "v31", "v32", "v33", "v34", "v35", "v36", "v37", "v38", "v39", "v40", "v41", "v42", "v43", "v44", "v45", "v46", "v47", "v48", "v49", "v50", "v51", "v52", "v53", "v54", "v55", "v56", "v57", "v58", "v59", "v60", "v61", "v62", "v63", "v64", "v65", "v66", "v67", "v68", "v69", "v70", "v71", "v72", "v73", "v74", "v75", "v76", "v77", "v78", "v79", "v80", "v81", "v82", "v83", "v84", "v85", "v86", "v87", "v88", "v89", "v90", "v91", "v92", "v93", "v94", "v95", "v96", "v97", "v98", "v99", "v100", "v101", "v102", "v103", "v104", "v105", "v106", "v107", "v108", "v109", "v110", "v111", "v112", "v113", "v114", "v115", "v116", "v117", "v118", "v119", "v120", "v121", "v122", "v123", "v124", "v125", "v126", "v127", "v128", "v129", "v130", "v131", "v132", "v133", "v134", "v135", "v136", "v137", "v138", "v139", "v140", "v141", "v142", "v143", "v144", "v145", "v146", "v147", "v148", "v149", "v150", "v151", "v152", "v153", "v154", "v155", "v156", "v157", "v158", "v159", "s40", "s41", "s42", "s43", "s44", "s45", "s46", "s47", "s48", "s49", "s50", "s51", "s52", "s53", "s54", "s55", "s56", "s57", "s58", "s59", "s60", "s61", "s62", "s63", "s64", "s65", "s66", "s67", "s68", "s69", "s70", "s71", "s72", "s73", "s74", "s75", "s76", "s77", "s78", "s79", "s80", "s81", "s82", "s83", "s84", "s85", "s86", "s87", "s88", "s89", "s90", "s91", "s92", "s93", "s94", "s95", "vcc", "scc", "memory"

DI unsigned selw(int p, int l15, int g) {
    const bool t = (p >> 1) == (l15 >> 3);
    return ((t && ((4 * g + ((2 * p) & 3)) & 7) == (l15 & 7)) ? 0x3F80u : 0u) | ((t && ((4 * g + ((2 * p + 1) & 3)) & 7) == (l15 & 7)) ? 0x3F800000u : 0u);
}
DI unsigned long long clk() { unsigned long long t; asm volatile("s_memtime %0\n\ts_waitcnt lgkmcnt(0)" : "=s"(t) :: "memory"); return t; }
DI void probe_q(rsrc_t rw, Tag tag, int cu, int lane) {
    unsigned best = 4095, x = 0;
    const unsigned base = W_PROBE + (cu & 3) * 8192 + (cu >> 2) * 1024;
    st4(rw, 0u, lane < NPR ? base + lane * 256 : OOB, 0); WAIT_VM(0);
#pragma unroll 1
    for (int r = 0; r < NPR; ++r) { const unsigned long long t0 = clk();
        x += __builtin_amdgcn_raw_buffer_load_b32(rw, base + r * 256 + (x & 0), 0, 16); WAIT_VM(0); asm volatile("" : "+v"(x)); best = min(best, (unsigned)(clk() - t0)); }
    if (cu == 0) { st4(rw, (unsigned)__builtin_amdgcn_s_memrealtime() & ~3u, F_Q + 8, 0); WAIT_VM(0); }
    if (lane == 0) set_flag(rw, tag, F_Q2 + cu * 128, best);
}
DI unsigned vote_q(rsrc_t rw, Tag tag, int lane) {
    const unsigned p = wait_flags(rw, tag, F_Q2, 128, 28, lane) * 32 + lane;
    int rank = 0;
    for (int k = 0; k < 28; ++k) rank += (unsigned)__builtin_amdgcn_readlane(p, k) < p;
    const unsigned m = (unsigned)__ballot(lane < 28 && rank < 7);
    const int c0 = __popc(m & 0x1111111u), c1 = __popc(m & 0x2222222u), c2 = __popc(m & 0x4444444u), c3 = __popc(m & 0x8888888u);
    return c0 + c2 >= c1 + c3 ? (c0 >= c2 ? 0 : 2) : (c1 >= c3 ? 1 : 3);
}

DI void chain_role(rsrc_t rw, Tag tag, int cu, unsigned ldsb) {
    const Args a = get_args();
    const int lane = fresh_lane(), w = RFL(threadIdx.x >> 6), tid = w * 64 + lane;
    const int h = cu >> 1, half = cu & 1, v = cu < 28 ? cu : cu - 4;
    const int l15 = lane & 15, g = lane >> 4, tp = (lane >> 3) & 1, i8 = lane & 7;
    if (cu < 28 && w == 0) probe_q(rw, tag, cu, lane);
    for (int i = 0; i < (cu < 28 ? 0 : 3); ++i) __builtin_amdgcn_s_sleep(16);
    BAR();
    unsigned warm = 0;
    {
        const rsrc_t r0 = __builtin_amdgcn_make_buffer_rsrc((void*)a.wv, 0, 448 * 8192, 0x00020000), r1 = __builtin_amdgcn_make_buffer_rsrc((void*)a.wo, 0, 448 * 8192, 0x00020000);
#pragma unroll
        for (int k = 0; k < 4; ++k) warm ^= __builtin_amdgcn_raw_buffer_load_b32(r0, tid * 128 + (k == 3 && tid >= 128 ? OOB : 0), cu * 114688 + k * 32768, 0);
#pragma unroll
        for (int k = 0; k < WWO; ++k) warm ^= __builtin_amdgcn_raw_buffer_load_b32(r1, tid * 128 + (k == 3 && tid >= 128 ? OOB : 0), cu * 114688 + k * 32768, 0);
    }
    constexpr unsigned C_RED = 14336, C_ORED = 16384, C_BK = 18432, C_VW = 19456, C_TBL = 76800;
    LAS unsigned* tb = (LAS unsigned*)(ldsb + C_TBL) + tid;
    const unsigned mk = (i8 & 1) ? 0xFFFF0000u : 0x0000FFFFu;
    tb[0 * 256] = (w > 0 ? (w - 1) * 2048 : 0) + lane * 16;
    tb[1 * 256] = 6144 + lane * 16;
    tb[2 * 256] = ldsb + lane * 16;
    tb[3 * 256] = ldsb + lane * 16 + (w & 1) * 4096;
    tb[4 * 256] = ldsb + lane * 16 + (w & 1) * 3072;
    { const int t0 = 14 * half + 3 * w + tp, t1 = tp == 0 ? 14 * half + 3 * w + 2 : 14 * half + 12 + (w >> 1);
      tb[5 * 256] = ldsb + 256 * ((t0 & ~1) + (g >> 1)) + i8 * 16 + (2 * (g & 1) + (t0 & 1)) * 4;
      tb[6 * 256] = ldsb + 256 * ((t1 & ~1) + (g >> 1)) + i8 * 16 + (2 * (g & 1) + (t1 & 1)) * 4; }
    tb[7 * 256] = ldsb + C_RED + (w * 2 + tp) * 256 + i8 * 32 + (g & 1) * 16;
    tb[8 * 256] = ldsb + C_RED + lane * 4;
    tb[9 * 256] = ldsb + C_ORED + w * 512 + i8 * 64 + g * 16;
    tb[10 * 256] = ldsb + C_ORED + ((lane >> 2) & 7) * 64 + ((lane & 3) * 4 + 2 * (lane >> 5)) * 4;
    tb[11 * 256] = ldsb + C_VW + w * 14336 + lane * 16;
    tb[12 * 256] = cu * 256 + lane * 4;
    tb[13 * 256] = (4 * w + g) * 512 + i8 * 32 + tp * 16;
    tb[14 * 256] = 7168 + (4 * w + g) * 64 + i8 * 8;
    tb[15 * 256] = ((v >> 1) << 14) + (v & 1) * 4096 + (4 * w + g) * 256 + l15 * 16;
    tb[16 * 256] = v * 256 + lane * 4;
    tb[17 * 256] = (lane & 8) ? 0xEEEE : 0x4444;
    tb[18 * 256] = ldsb + C_BK;
    tb[19 * 256] = lane == 0 ? (4 + (cu & 1)) * 32768 : OOB;
#pragma unroll
    for (int p = 0; p < 4; ++p) { tb[(20 + p) * 256] = selw(p, l15, g); tb[(24 + p) * 256] = (i8 >> 1) == p ? mk : 0u; }
    tb[28 * 256] = lane * 16;
    tb[29 * 256] = lane < 56 ? F_W + (224 + w * 56 + lane) * 64 : F_RDY + (8 * w + lane - 56) * 64;
    if (w == 0) *(LAS unsigned*)(ldsb + LDS_BYTES - 16) = vote_q(rw, tag, lane);
    BAR();
    const unsigned q = RFL(*(LAS unsigned*)(ldsb + LDS_BYTES - 16)), qoff = q * 8192, pol = (q & 1) * 8192;
    {
        const unsigned o = v * 256 + lane * 4, sl = w * 32768;
        const v4u s4 = {SENT, SENT, SENT, SENT};
        st4(rw, SENT, cu * 256 + lane * 4, W_S + sl + qoff);
        if (cu == 0) st16(rw, s4, 7168 + lane * 16, sl + qoff);
        if (cu < 28) {
            st4(rw, SENT, o, sl + qoff); st4(rw, SENT, o, W_MX + sl + (qoff ^ 16384));
#pragma unroll
            for (int j = 0; j < 4; ++j) { st16(rw, s4, ((v >> 1) << 14) + (v & 1) * 4096 + j * 1024 + lane * 16, W_O + w * (2 * 28 * 4096) + pol);
                st16(rw, s4, g * 8192 + v * 256 + l15 * 16, W_MP + (w * 16 + j * 4) * 8192); }
            if (w == 0) {
                const int r = lane >> 2, c = 16 * v + 4 * (lane & 3) + 2 * (r >> 3);
                const unsigned val = min(*(const unsigned*)(a.mem0 + (r & 7) * 448 + c), 0xFFFFFFFEu);
                WAIT_VM(0);
                st4(rw, val, o, qoff); st4(rw, val, o, W_MX + (qoff ^ 16384)); st4(rw, val, o, W_MP);
            }
        }
        publish(rw, tag, F_RDY + cu * 64);
    }
    asm volatile("" :: "v"(warm));
    wait_flags(rw, tag, F_W + h * 14 * 64, 64, 14, lane);
    const unsigned t_org = RFL(__builtin_amdgcn_raw_buffer_load_b32(rw, F_Q + 8, 0, 16));
    const unsigned sq0 = O_WF + (2 * h) * COMBO + (14 * half + 3 * w) * 14336;
    const unsigned sq3 = O_WF + (2 * h) * COMBO + (14 * half + 12 + (w >> 1)) * 14336 + (w & 1) * 8192;
    const unsigned sv0 = O_WF + (8 * w + 1) * COMBO + v * 14336;
    asm volatile(CHAIN_ASM :: "s"(rw), "s"(RFL(ldsb + C_TBL + w * 256)), "s"(w), "s"(qoff), "s"(pol), "s"(RFL(sq0)), "s"(RFL(sq3)), "s"(RFL(sv0)),
                 "s"(RFL(ldsb + (w > 0 ? (w - 1) * 2048 : 0))), "s"(RFL(ldsb + 6144)), "s"(RFL(((unsigned)(27 - cu) >> 31) ^ 1)), "s"(RFL(F_STAT + (cu * 4 + w) * 16)), "s"(RFL((unsigned)(cu >= 30) & (unsigned)(w == 1))), "s"(tag.lo), "s"(tag.hi), "s"(t_org) : CHAIN_CLOB);
    if (cu == 0 && w == 0 && fresh_lane() == 0) set_flag(rw, tag, F_GEN, 0);
}
DI void x_role(rsrc_t rw, Tag tag, int xi, unsigned ldsb) {
    const int lane = fresh_lane(), w = RFL(threadIdx.x >> 6), tid = w * 64 + lane;
    const int h = xi >> 1, ch = xi & 1, l15 = lane & 15, g = lane >> 4;
    LAS unsigned* tb = (LAS unsigned*)(ldsb + 8192) + tid;
    const int ct0 = ch * 14 + (w < 3 ? 4 * w : 11), c1 = ct0 + g, c2 = ct0 + 2 + (g & 1);
    tb[0 * 256] = (2 * w) * 1024 + lane * 16;
    tb[1 * 256] = (w == 3 ? 6 : 2 * w + 1) * 1024 + lane * 16;
    tb[2 * 256] = (4 * w) * 1024 + lane * 16;
    tb[3 * 256] = (w == 3 ? 12 : 4 * w + 2) * 1024 + lane * 16;
    tb[4 * 256] = ct0 * 1024 + lane * 16;
    tb[5 * 256] = ldsb + w * 1024 + lane * 16;
    tb[6 * 256] = ldsb + lane * 16;
    tb[7 * 256] = 7168 + h * 64 + (lane & 7) * 8;
    tb[8 * 256] = (w >= 2 && g == 3) ? OOB : ((c1 >> 1) << 14) + (c1 & 1) * 4096 + h * 256 + l15 * 16;
    tb[9 * 256] = (lane & 8) ? 0xEEEE : 0x4444;
    tb[10 * 256] = w == 3 ? 0u : 0xFFFFFFFFu;
    tb[11 * 256] = (w >= 2 && (g & 1)) ? OOB : ((c2 >> 1) << 14) + (c2 & 1) * 4096 + h * 256 + l15 * 16;
    tb[12 * 256] = F_GU + (2 * h + (lane & 1)) * 64;
    tb[13 * 256] = (4 + (xi >> 4)) * 32768;
#pragma unroll
    for (int p = 0; p < 4; ++p) tb[(14 + p) * 256] = selw(p, l15, g);
    if (w == 0) *(LAS unsigned*)(ldsb + LDS_BYTES - 16) = vote_q(rw, tag, lane);
    BAR();
    const unsigned q = RFL(*(LAS unsigned*)(ldsb + LDS_BYTES - 16)), qoff = q * 8192, pol = (q & 1) * 8192;
    {
        unsigned acc = 0;
#pragma unroll
        for (int k = 0; k < 4; ++k) acc ^= __builtin_amdgcn_raw_buffer_load_b32(rw, min((unsigned)(k * 256 + tid) * 65536u, O_P - O_G), O_G, 16);
        asm volatile("" :: "v"(acc));
    }
    wait_flags(rw, tag, F_RDY, 64, 32, lane);
    __syncthreads();
    const unsigned long long mlw = (w == 0 && ch == 0) ? 0xFFull : 0ull;
    asm volatile(X_ASM :: "s"(rw), "s"(RFL(ldsb + 8192 + w * 256)), "s"(RFL((unsigned)(xi == 31) & (unsigned)(w == 3))), "s"(qoff), "s"(pol), "s"(RFL(O_G + h * SEGH)), "s"(RFL(O_U + h * SEGH)), "s"(mlw),
                 "s"(RFL(F_STAT + ((32 + xi) * 4 + w) * 16)), "s"(tag.lo), "s"(tag.hi) : X_CLOB);
}

__global__ void __launch_bounds__(256, 1) mega(Args a) {
    extern __shared__ __attribute__((aligned(16))) char lds[];
    const unsigned ldsb = (unsigned)(size_t)lds;
    const int b = blockIdx.x, x = (b + ROT) & 7, r = b >> 3;
    const rsrc_t rw = __builtin_amdgcn_make_buffer_rsrc(a.ws, 0, O_END, 0x00020000);
    unsigned sink = 0;
    {
        unsigned long long pc; unsigned len;
        asm volatile("s_getpc_b64 %0\nCPF_%=:\n\ts_mov_b32 %1, MEGA_CHAIN_END-CPF_%=" : "=s"(pc), "=s"(len));
        const rsrc_t rc = __builtin_amdgcn_make_buffer_rsrc((void*)pc, 0, len & ~3u, 0x00020000);
        asm volatile("buffer_load_dword %0, %1, %2, 0 offen" : "=v"(sink) : "v"(threadIdx.x < 48 ? (r * 48 + threadIdx.x) * 64 : OOB), "s"(rc) : "memory");
    }
    unsigned sink2 = 0;
    v2u gen;
    asm volatile("buffer_load_dwordx2 %0, %1, %2, 0 offen sc1" : "=v"(gen) : "v"(F_GEN), "s"(rw) : "memory");
    const int fv = (x - 1) * 32 + r, fh = fv / 14, fk = fv % 14 >= 7 ? 7 : 0, fn = (fv % 7) * 4;
    if (x != 0) fold_start(fh, fk, fn, ldsb);
    asm volatile("s_cmp_eq_u32 %3, 0\n\ts_cbranch_scc1 GEN0_%=\n\ts_waitcnt vmcnt(24)\n\ts_branch GEN1_%=\nGEN0_%=:\n\ts_waitcnt vmcnt(0)\nGEN1_%=:" : "+v"(gen), "+v"(sink), "+v"(sink2) : "s"(x) : "scc", "memory");
    const Tag tag = {(unsigned)RFL(gen.x) + 1u, (unsigned)RFL(gen.y) ^ 0x9E377000u};
    if (r == 0 && threadIdx.x == 0) set_flag(rw, tag, F_X + x * 64, (unsigned)__builtin_amdgcn_s_getreg(20 | (3 << 11)) & 15u);
    if (x == 0) chain_role(rw, tag, r, ldsb);
    else {
        fold_pass<false>(rw, fh, fk, fn, ldsb);
        publish(rw, tag, F_W + fv * 64);
        const unsigned xc = wait_flags(rw, tag, F_X, 64, 8, fresh_lane()) >> 2;
        const unsigned same = (unsigned)__ballot(xc == RFL(xc)) & 0xFEu;
        fold_pass<true>(rw, fh, fk, fn, ldsb);
        publish(rw, tag, F_W + (224 + fv) * 64);
        const unsigned cand = __popc(same) >= 2 ? same : 0xFEu, j1 = __ffs(cand) - 1, j2 = __ffs(cand & (cand - 1)) - 1;
        const bool side = x == j1 || x == j2;
        const int k = (x == j2) * 16 + (r & 15), v = (x - 1 - (x > j1) - (x > j2)) * 32 + r, combo = v / 5, rr = v % 5;
        if (side && r < 16) x_role(rw, tag, k, ldsb);
        else if (side || rr == 4) {
            fft_tables(rw, tag, side ? 32 + k : combo, ldsb);
            if (!side) { fft_role(rw, tag, combo, ldsb); BAR(); int c2 = combo; asm volatile("" : "+s"(c2)); pv_role(rw, tag, c2, ldsb); }
        }
        else if (combo & 1) worker_body<false>(rw, tag, combo, rr, ldsb);
        else worker_body<true>(rw, tag, combo, rr, ldsb);
    }
}

extern "C" void kernel_launch(void* const* d_in, const int* in_sizes, int n_in, void* d_out, int out_size, void* d_ws, size_t ws_size, hipStream_t stream) {
    Args a;
    a.ax = (const u16*)d_in[0]; a.wr = (const u16*)d_in[1]; a.wi = (const u16*)d_in[2]; a.bx = (const u16*)d_in[3]; a.mem0 = (const u16*)d_in[4];
    a.wq = (const u16*)d_in[5]; a.wk = (const u16*)d_in[6]; a.wv = (const u16*)d_in[7]; a.wo = (const u16*)d_in[8];
    a.out = (u16*)d_out;
    a.ws = (char*)(((size_t)d_ws + (2u << 20) - 1) & ~(size_t)((2u << 20) - 1));
    (void)hipFuncSetAttribute((const void*)mega, hipFuncAttributeMaxDynamicSharedMemorySize, LDS_BYTES);
    mega<<<256, 256, LDS_BYTES, stream>>>(a);
}
```
